```python
import torch

def _softmax(x, axis=-1):
    return torch.softmax(x, dim=axis)
def _gelu(x):
    return torch.nn.functional.gelu(x, approximate="tanh")
def _silu(x):
    return torch.nn.functional.silu(x)
def _sigmoid(x):
    return torch.sigmoid(x)
def _layernorm(x, axis=-1, eps=1e-5):
    m = x.mean(dim=axis, keepdim=True)
    v = x.var(dim=axis, keepdim=True, unbiased=False)
    return (x - m) / torch.sqrt(v + eps)
def _scatter_add(tgt, idx, src, axis=0):
    return tgt.clone().index_add_(axis, idx, src)
def _segment_sum(src, seg, n):
    out = torch.zeros((n,) + src.shape[1:], dtype=src.dtype)
    return out.index_add_(0, seg, src)
def _pad(x, pw):
    flat = tuple(v for pair in reversed(pw) for v in pair)
    return torch.nn.functional.pad(x, flat)

kernel_name = 'interleave_layers__sliding_block__linear_attn_block__prompt'

def setup_inputs():

    _w = (lambda a: (lambda t: t.to(torch.bfloat16) if t.is_floating_point() else t)(torch.as_tensor(a)))
    def _A_setup():
        B, S, D = 1, 2048, 512
        return {
            "x": _w((lambda *s: torch.randn(*s, dtype=torch.bfloat16) * 0.05)(B, S, D)),
            "wqkv": _w((lambda *s: torch.randn(*s, dtype=torch.bfloat16) * 0.1)(D, 3 * D)),
            "wo": _w((lambda *s: torch.randn(*s, dtype=torch.bfloat16) * 0.1)(D, D)),
        }
    _A_in = _A_setup()

    def _B_setup():
        B, S, D = 1, 2048, 512
        return {
            "x": _w((lambda *s: torch.randn(*s, dtype=torch.bfloat16) * 0.05)(B, S, D)),
            "wqkv": _w((lambda *s: torch.randn(*s, dtype=torch.bfloat16) * 1.6)(D, 3 * D)),
            "wo": _w((lambda *s: torch.randn(*s, dtype=torch.bfloat16) * 0.1)(D, D)),
        }
    _B_in = _B_setup()

    return {
        "A_x": _A_in["x"],
        "A_wqkv": _A_in["wqkv"],
        "A_wo": _A_in["wo"],
        "B_x": _B_in["x"],
        "B_wqkv": _B_in["wqkv"],
        "B_wo": _B_in["wo"],
    }

def reference(A_x, A_wqkv, A_wo, B_x, B_wqkv, B_wo):
    def _A_ref(x, wqkv, wo):
        B, S, D = x.shape
        H = 32; Dh = D // H; W = 256
        qkv = (lambda x, *d: x.permute(*d))((x @ wqkv).reshape(B, S, 3, H, Dh), 2, 0, 3, 1, 4)
        q, k, v = qkv[0], qkv[1], qkv[2]
        scores = torch.einsum("bhqd,bhkd->bhqk", q, k) * (Dh ** -0.5)
        idx = torch.arange(S)
        mask = (idx[:, None] - idx[None, :] >= 0) & (idx[:, None] - idx[None, :] < W)
        scores = torch.where(mask[None, None], scores, -1e9)
        p = _softmax(scores, axis=-1)
        o = torch.einsum("bhqk,bhkd->bhqd", p, v)
        return (lambda x, *d: x.permute(*d))(o, 0, 2, 1, 3).reshape(B, S, D) @ wo

    def _B_ref(x, wqkv, wo):
        B, S, D = x.shape
        H = 32; Dh = D // H
        qkv = (lambda x, *d: x.permute(*d))((x @ wqkv).reshape(B, S, 3, H, Dh), 2, 0, 3, 1, 4)
        q, k, v = qkv[0], qkv[1], qkv[2]
        phi = lambda t: torch.where(t > 0, t + 1, torch.exp(t)) + 1e-6
        pq, pk = phi(q), phi(k)
        kv = torch.einsum("bhsd,bhse->bhde", pk, v)
        z = torch.einsum("bhsd->bhd", pk)
        num = torch.einsum("bhqd,bhde->bhqe", pq, kv)
        den = torch.einsum("bhqd,bhd->bhq", pq, z)[..., None] + 1e-6
        o = num / den
        return (lambda x, *d: x.permute(*d))(o, 0, 2, 1, 3).reshape(B, S, D) @ wo

    x = A_x
    x = x + _A_ref(_layernorm(x, axis=-1), A_wqkv, A_wo)
    x = x + _B_ref(_layernorm(x, axis=-1), B_wqkv, B_wo)
    x = x + _A_ref(_layernorm(x, axis=-1), A_wqkv, A_wo)
    x = x + _B_ref(_layernorm(x, axis=-1), B_wqkv, B_wo)
    return x
```

```cpp
#include <hip/hip_runtime.h>
#include <hip/hip_bf16.h>

typedef __bf16 bf16x8 __attribute__((ext_vector_type(8)));
typedef __bf16 bf16x2 __attribute__((ext_vector_type(2)));
typedef float f32x16 __attribute__((ext_vector_type(16)));
typedef float f32x4 __attribute__((ext_vector_type(4)));
typedef unsigned u32x4 __attribute__((ext_vector_type(4)));
typedef unsigned u32x2 __attribute__((ext_vector_type(2)));
#define GAS __attribute__((address_space(1)))
#define LAS __attribute__((address_space(3)))
typedef GAS unsigned long long gu64;
#define DI __device__ __forceinline__
#define RLX_AGENT __ATOMIC_RELAXED, __HIP_MEMORY_SCOPE_AGENT
#define CBAR() asm volatile("" ::: "memory")

constexpr unsigned OFF_FLAGS = 4096;
constexpr unsigned OFF_W = 1u << 20;
constexpr unsigned SZ_W = 1572864;
constexpr unsigned OFF_WO = OFF_W + 2 * SZ_W;
constexpr unsigned SZ_WO = 524288;
constexpr unsigned OFF_CSP = OFF_WO + 2 * SZ_WO;
constexpr unsigned SZ_CSP = 16 * 32 * 384;
constexpr unsigned OFF_X = 6u << 20;
constexpr unsigned OFF_ST = 12u << 20;
constexpr unsigned OFF_KV = 13u << 20;
constexpr unsigned OFF_O = 21u << 20;
constexpr unsigned OFF_KVP = 29u << 20;
constexpr unsigned SZ_KVP = 16 * 16 * 2304;
constexpr unsigned OFF_KVF = 31u << 20;
constexpr unsigned SZ_KVF = 32 * 640;
constexpr unsigned WS_BYTES = 32u << 20;
constexpr unsigned OOB = 0x80000000u;

constexpr int L_W = 0, L_WO = 98304, L_S = 131072;
constexpr int L_QKV = L_S, L_MH = L_S + 24576, L_STAT = L_S + 25600, L_CS = L_S + 26624;
constexpr int L_RED = L_S, L_KVF = L_S;
constexpr int LDS_BYTES = L_S + 27648;

constexpr float QSCALE = 0.25f * 1.4426950408889634f;
constexpr float LOG2E = 1.4426950408889634f;
constexpr unsigned SPIN_CAP = 1u << 22;

struct Ctx {
    __amdgpu_buffer_rsrc_t ws;
    unsigned char* wsp;
    unsigned char* lds;
    unsigned long long epoch;
    int t, c, u, lane, hi, cu;
    int t0, c0, u0;
};

DI f32x16 mfma(u32x4 a, u32x4 b, f32x16 c) {
    return __builtin_amdgcn_mfma_f32_32x32x16_bf16(__builtin_bit_cast(bf16x8, a), __builtin_bit_cast(bf16x8, b), c, 0, 0, 0);
}
DI unsigned pk2(float a, float b) { bf16x2 v = {(__bf16)a, (__bf16)b}; return __builtin_bit_cast(unsigned, v); }
DI float bflo(unsigned x) { return __uint_as_float(x << 16); }
DI float bfhi(unsigned x) { return __uint_as_float(x & 0xffff0000u); }
DI float xhalf_sum(float x) { auto r = __builtin_amdgcn_permlane32_swap(__float_as_uint(x), __float_as_uint(x), false, false); return __uint_as_float(r[0]) + __uint_as_float(r[1]); }
DI float xhalf_max(float x) { auto r = __builtin_amdgcn_permlane32_swap(__float_as_uint(x), __float_as_uint(x), false, false); return fmaxf(__uint_as_float(r[0]), __uint_as_float(r[1])); }
DI u32x4 ldw(const Ctx& X, unsigned voff, unsigned soff) { return __builtin_amdgcn_raw_buffer_load_b128(X.ws, voff, soff, 16); }
DI void stw(const Ctx& X, u32x4 v, unsigned voff, unsigned soff) { __builtin_amdgcn_raw_buffer_store_b128(v, X.ws, voff, soff, 16); }
DI u32x4 lds128(const Ctx& X, int off) { return *(const u32x4*)(X.lds + off); }
DI f32x4 lds128f(const Ctx& X, int off) { return *(const f32x4*)(X.lds + off); }
DI void bar() { asm volatile("s_waitcnt lgkmcnt(0)\n\ts_barrier" ::: "memory"); }
DI void drain() { asm volatile("s_waitcnt vmcnt(0)" ::: "memory"); __builtin_amdgcn_s_waitcnt(0x0F70); }
DI f32x16 zero16() { f32x16 z; _Pragma("unroll") for (int i = 0; i < 16; ++i) z[i] = 0.f; return z; }

DI void fresh(Ctx& X) {
    X.lane = __builtin_amdgcn_mbcnt_hi(~0u, __builtin_amdgcn_mbcnt_lo(~0u, 0u));
    asm volatile("" : "+v"(X.lane));
    X.t = X.t0; X.c = X.c0; X.u = X.u0;
    asm volatile("" : "+s"(X.t), "+s"(X.c), "+s"(X.u));
    X.hi = X.lane >> 5; X.cu = X.t * 16 + X.c;
}
DI void ld32(const Ctx& X, u32x4 (&xf)[32], unsigned base) {
#pragma unroll
    for (int s = 0; s < 32; ++s) xf[s] = ldw(X, X.lane * 16 + (s & 3) * 1024, base + (s >> 2) * 4096);
}
DI void publish(const Ctx& X, int hop) {
    drain();
    bar();
    if (X.u == 0 && X.lane == 0) __hip_atomic_store((gu64*)(X.wsp + OFF_FLAGS + (hop * 256 + X.cu) * 128), X.epoch, RLX_AGENT);
    CBAR();
}
DI void waitflags(const Ctx& X, int hop, int first, int stride, int n) {
    CBAR();
    const gu64* f = (const gu64*)(X.wsp + OFF_FLAGS + (hop * 256 + first + X.lane * stride) * 128);
    for (unsigned spins = 0; spins < SPIN_CAP; ++spins) {
        unsigned long long v = X.epoch;
        if (X.lane < n) v = __hip_atomic_load(f, RLX_AGENT);
        if (__all(v == X.epoch)) break;
        __builtin_amdgcn_s_sleep(1);
    }
    CBAR();
}
DI void glds_copy(const Ctx& X, unsigned goff, int loff, int pieces_per_wave) {
    const unsigned char* g = X.wsp + goff + X.u * pieces_per_wave * 1024 + X.lane * 16;
    unsigned char* l = X.lds + loff + X.u * pieces_per_wave * 1024;
    for (int i = 0; i < pieces_per_wave; ++i)
        __builtin_amdgcn_global_load_lds((const GAS void*)(g + i * 1024), (LAS void*)(l + i * 1024), 16, 0, 16);
}

template <bool LA>
DI void gemm_qkv(const Ctx& X, const u32x4 (&xf)[32], f32x16& a0, f32x16& a1, f32x16& a2) {
    a0 = zero16(); a1 = zero16(); a2 = zero16();
    u32x4 w[3][3];
    const int o = L_W + X.lane * 16;
#pragma unroll
    for (int s = 0; s < 2; ++s)
#pragma unroll
        for (int i = 0; i < 3; ++i) w[s][i] = lds128(X, o + s * 3072 + i * 1024);
#pragma unroll
    for (int s = 0; s < 32; ++s) {
        if (s + 2 < 32) {
#pragma unroll
            for (int i = 0; i < 3; ++i) w[(s + 2) % 3][i] = lds128(X, o + (s + 2) * 3072 + i * 1024);
        }
        a0 = mfma(w[s % 3][0], xf[s], a0);
        if (LA) a1 = mfma(w[s % 3][1], xf[s], a1); else a1 = mfma(xf[s], w[s % 3][1], a1);
        a2 = mfma(xf[s], w[s % 3][2], a2);
        asm volatile("" : "+v"(a0), "+v"(a1), "+v"(a2));
        __builtin_amdgcn_sched_barrier(0);
    }
}
DI void finish_stats(const Ctx& X, float s1, float s2, float& ra, float& rb) {
    const float mean = s1 * (1.f / 512.f);
    const float var = fmaxf(s2 * (1.f / 512.f) - mean * mean, 0.f);
    ra = __builtin_amdgcn_rsqf(var + 1e-5f);
    rb = -mean * ra;
    if (X.lane < 32) {
        *(float*)(X.lds + L_STAT + X.u * 256 + X.lane * 4) = ra;
        *(float*)(X.lds + L_STAT + X.u * 256 + 128 + X.lane * 4) = rb;
    }
}
DI void ln_fix_T(const Ctx& X, f32x16& a, int csoff, float ra, float rb) {
#pragma unroll
    for (int g = 0; g < 4; ++g) {
        f32x4 cs = lds128f(X, csoff + (8 * g + 4 * X.hi) * 4);
#pragma unroll
        for (int i = 0; i < 4; ++i) a[4 * g + i] = fmaf(ra, a[4 * g + i], rb * cs[i]);
    }
}
DI void ln_fix_N(const Ctx& X, f32x16& a, int csoff) {
    const float cs = *(const float*)(X.lds + csoff + (X.lane & 31) * 4);
#pragma unroll
    for (int g = 0; g < 4; ++g) {
        f32x4 ra = lds128f(X, L_STAT + X.u * 256 + (8 * g + 4 * X.hi) * 4);
        f32x4 rb = lds128f(X, L_STAT + X.u * 256 + 128 + (8 * g + 4 * X.hi) * 4);
#pragma unroll
        for (int i = 0; i < 4; ++i) a[4 * g + i] = fmaf(ra[i], a[4 * g + i], rb[i] * cs);
    }
}
DI u32x4 pack8(const f32x16& a, int b) { u32x4 r = {pk2(a[b], a[b + 1]), pk2(a[b + 2], a[b + 3]), pk2(a[b + 4], a[b + 5]), pk2(a[b + 6], a[b + 7])}; return r; }

DI void load_x(const Ctx& X, int li, u32x4 (&xf)[32], float& ra, float& rb) {
    const unsigned tile = 4 * X.t + X.u;
    u32x2 st[8];
#pragma unroll
    for (int i = 0; i < 8; ++i)
        st[i] = __builtin_amdgcn_raw_buffer_load_b64(X.ws, (X.hi * 8 + i) * 256 + (X.lane & 31) * 8, OFF_ST + li * 262144 + tile * 4096, 16);
    ld32(X, xf, OFF_X + li * (2u << 20) + tile * 32768);
    CBAR();
    float s1 = 0.f, s2 = 0.f;
#pragma unroll
    for (int i = 0; i < 8; ++i) { s1 += __uint_as_float(st[i][0]); s2 += __uint_as_float(st[i][1]); }
    finish_stats(X, xhalf_sum(s1), xhalf_sum(s2), ra, rb);
}

constexpr unsigned long long diag_mask(int r) {
    unsigned long long m = 0;
    for (int l = 0; l < 64; ++l) if ((r & 3) + 8 * (r >> 2) + 4 * (l >> 5) <= (l & 31)) m |= 1ull << l;
    return m;
}
struct KVt { u32x4 k, v0, v1; };

DI void exp_pack(const f32x16& s, u32x4& p0, u32x4& p1) {
    f32x16 e;
#pragma unroll
    for (int i = 0; i < 16; ++i) e[i] = __builtin_amdgcn_exp2f(s[i]);
    p0 = pack8(e, 0); p1 = pack8(e, 8);
}
DI void att_full(const KVt& kv, u32x4 q, const f32x16& nm, f32x16& o) {
    f32x16 s = mfma(kv.k, q, nm);
    u32x4 p0, p1; exp_pack(s, p0, p1);
    o = mfma(kv.v0, p0, o); o = mfma(kv.v1, p1, o);
}
template <int R> DI float selm(float d, float f) { return __builtin_amdgcn_inverse_ballot_w64(diag_mask(R)) ? d : f; }
template <int I> DI unsigned pmask() {
    return (__builtin_amdgcn_inverse_ballot_w64(diag_mask(2 * I)) ? 0xffffu : 0u) | (__builtin_amdgcn_inverse_ballot_w64(diag_mask(2 * I + 1)) ? 0xffff0000u : 0u);
}
DI void att_merged(const KVt& dg, const KVt& fr, u32x4 q, const f32x16& nm, f32x16& o) {
    f32x16 sd = mfma(dg.k, q, nm), sf = mfma(fr.k, q, nm), s;
    s[0] = selm<0>(sd[0], sf[0]); s[1] = selm<1>(sd[1], sf[1]); s[2] = selm<2>(sd[2], sf[2]); s[3] = selm<3>(sd[3], sf[3]);
    s[4] = selm<4>(sd[4], sf[4]); s[5] = selm<5>(sd[5], sf[5]); s[6] = selm<6>(sd[6], sf[6]); s[7] = selm<7>(sd[7], sf[7]);
    s[8] = selm<8>(sd[8], sf[8]); s[9] = selm<9>(sd[9], sf[9]); s[10] = selm<10>(sd[10], sf[10]); s[11] = selm<11>(sd[11], sf[11]);
    s[12] = selm<12>(sd[12], sf[12]); s[13] = selm<13>(sd[13], sf[13]); s[14] = selm<14>(sd[14], sf[14]); s[15] = selm<15>(sd[15], sf[15]);
    u32x4 p0, p1; exp_pack(s, p0, p1);
    const u32x4 m0 = {pmask<0>(), pmask<1>(), pmask<2>(), pmask<3>()}, m1 = {pmask<4>(), pmask<5>(), pmask<6>(), pmask<7>()};
    const u32x4 d0 = p0 & m0, d1 = p1 & m1;
    o = mfma(dg.v0, d0, o); o = mfma(dg.v1, d1, o);
    o = mfma(fr.v0, p0 ^ d0, o); o = mfma(fr.v1, p1 ^ d1, o);
}
DI KVt own_kv(const Ctx& X, int e, int hd) {
    KVt r; const int b = L_QKV + (e * 2 + hd) * 3072;
    r.k = lds128(X, b + 1024 + X.lane * 16);
    const u32x4 ones = {0x3f803f80u, 0x3f803f80u, 0x3f803f80u, 0x3f803f80u};
    r.v0 = ones; r.v1 = ones;
    if (!(X.lane & 16)) { const int o = b + 2048 + (X.hi * 16 + (X.lane & 15)) * 16; r.v0 = lds128(X, o); r.v1 = lds128(X, o + 512); }
    return r;
}
DI KVt glob_kv(const Ctx& X, int la, int gt, int head) {
    KVt r; const bool ok = gt >= 0;
    const unsigned so = OFF_KV + la * (4u << 20) + (ok ? (unsigned)(gt * 32 + head) * 2048u : 0u);
    const unsigned ob = ok ? 0u : OOB;
    r.k = ldw(X, X.lane * 16 + ob, so);
    const unsigned one = ok ? 0x3f803f80u : 0u;
    const u32x4 ones = {one, one, one, one};
    r.v0 = ones; r.v1 = ones;
    if (!(X.lane & 16)) { const unsigned o = 1024 + (X.hi * 16 + (X.lane & 15)) * 16 + ob; r.v0 = ldw(X, o, so); r.v1 = ldw(X, o + 512, so); }
    return r;
}
DI void att_slow(const Ctx& X, int la, int gq, int head, u32x4 q, f32x16& o) {
    const int qi = X.lane & 31;
    float m = -INFINITY;
    for (int d = 0; d <= 8; ++d) {
        if (gq - d < 0) break;
        KVt kv = glob_kv(X, la, gq - d, head);
        f32x16 s = mfma(kv.k, q, zero16());
#pragma unroll
        for (int r = 0; r < 16; ++r) { const int dist = qi - ((r & 3) + 8 * (r >> 2) + 4 * X.hi) + 32 * d; if (dist >= 0 && dist < 256) m = fmaxf(m, s[r]); }
    }
    m = xhalf_max(m);
    o = zero16();
    for (int d = 0; d <= 8; ++d) {
        if (gq - d < 0) break;
        KVt kv = glob_kv(X, la, gq - d, head);
        f32x16 s = mfma(kv.k, q, zero16());
#pragma unroll
        for (int r = 0; r < 16; ++r) { const int dist = qi - ((r & 3) + 8 * (r >> 2) + 4 * X.hi) + 32 * d; s[r] = (dist >= 0 && dist < 256) ? s[r] - m : -INFINITY; }
        u32x4 p0, p1; exp_pack(s, p0, p1);
        o = mfma(kv.v0, p0, o); o = mfma(kv.v1, p1, o);
    }
}
DI void att_finish(const Ctx& X, int la, int q, int hd, u32x4 qf, f32x16& o) {
    const int head = 2 * X.c + hd, gq = 4 * X.t + q;
    float mx = 0.f;
#pragma unroll
    for (int i = 0; i < 8; ++i) mx = fmaxf(mx, fabsf(o[i]));
    bool bad = !(o[8] < 0x1p100f) || !(o[8] > 0x1p-20f) || !(mx < 0x1p120f);
#pragma unroll
    for (int i = 0; i < 8; ++i) bad |= (o[i] != o[i]);
    if (__any(bad)) att_slow(X, la, gq, head, qf, o);
    const float inv = __builtin_amdgcn_rcpf(o[8]);
    u32x4 r = {pk2(o[0] * inv, o[1] * inv), pk2(o[2] * inv, o[3] * inv), pk2(o[4] * inv, o[5] * inv), pk2(o[6] * inv, o[7] * inv)};
    stw(X, r, X.lane * 16, OFF_O + (2 * la) * (2u << 20) + (unsigned)(gq * 32 + head) * 1024u);
}
template <int QA, int QB>
DI void attention(const Ctx& X, int la) {
    const int hd = X.u & 1, head = 2 * X.c + hd;
    constexpr int NH = 8 - QA;
    KVt h[NH];
#pragma unroll
    for (int i = 0; i < NH; ++i) h[i] = glob_kv(X, la, 4 * X.t - 1 - i, head);
    CBAR();
    const u32x4 qa = lds128(X, L_QKV + (QA * 2 + hd) * 3072 + X.lane * 16), qb = lds128(X, L_QKV + (QB * 2 + hd) * 3072 + X.lane * 16);
    const float ma = -*(const float*)(X.lds + L_MH + (QA * 2 + hd) * 128 + (X.lane & 31) * 4);
    const float mb = -*(const float*)(X.lds + L_MH + (QB * 2 + hd) * 128 + (X.lane & 31) * 4);
    f32x16 na, nb, oa = zero16(), ob = zero16();
#pragma unroll
    for (int i = 0; i < 16; ++i) { na[i] = ma; nb[i] = mb; }
#pragma unroll
    for (int e = 0; e < QB; ++e) {
        KVt kv = own_kv(X, e, hd);
        att_full(kv, qb, nb, ob);
        if (e < QA) att_full(kv, qa, na, oa);
    }
#pragma unroll
    for (int i = 0; i < NH; ++i) {
        const int e = -1 - i;
        if (e > QA - 8) att_full(h[i], qa, na, oa);
        if (e == QA - 8) { KVt dg = own_kv(X, QA, hd); att_merged(dg, h[i], qa, na, oa); }
        if (e > QB - 8) att_full(h[i], qb, nb, ob);
        if (e == QB - 8) { KVt dg = own_kv(X, QB, hd); att_merged(dg, h[i], qb, nb, ob); }
    }
    att_finish(X, la, QA, hd, qa, oa);
    att_finish(X, la, QB, hd, qb, ob);
}

DI void outproj_finish(const Ctx& X, const f32x16& acc, u32x4 (&xr)[2], int li, bool last, unsigned char* out) {
    float s1 = 0.f, s2 = 0.f;
#pragma unroll
    for (int i = 0; i < 8; ++i) {
        const unsigned a = pk2(acc[2 * i], acc[2 * i + 1]);
        const unsigned old = xr[i >> 2][i & 3];
        const unsigned n = pk2(bflo(old) + bflo(a), bfhi(old) + bfhi(a));
        xr[i >> 2][i & 3] = n;
        const float lo = bflo(n), hi = bfhi(n);
        s1 += lo + hi; s2 = fmaf(lo, lo, fmaf(hi, hi, s2));
    }
    const unsigned tile = 4 * X.t + X.u;
    if (last) {
        u32x4* p = (u32x4*)(out + (size_t)(tile * 32 + (X.lane & 31)) * 1024 + X.c * 64 + X.hi * 32);
        p[0] = xr[0]; p[1] = xr[1];
    } else {
        stw(X, xr[0], X.lane * 16, OFF_X + li * (2u << 20) + tile * 32768 + X.c * 2048);
        stw(X, xr[1], X.lane * 16 + 1024, OFF_X + li * (2u << 20) + tile * 32768 + X.c * 2048);
        s1 = xhalf_sum(s1); s2 = xhalf_sum(s2);
        u32x2 st = {__float_as_uint(s1), __float_as_uint(s2)};
        if (X.lane < 32) __builtin_amdgcn_raw_buffer_store_b64(st, X.ws, X.lane * 8, OFF_ST + li * 262144 + tile * 4096 + X.c * 256, 16);
    }
}
DI float phi(float x) { return fmaxf(x, 0.f) + (__builtin_amdgcn_exp2f(fminf(x * LOG2E, 0.f)) + 1e-6f); }

__global__ __launch_bounds__(256, 2) void fused_kernel(const unsigned short* __restrict__ xin, const unsigned short* __restrict__ wA, const unsigned short* __restrict__ woA,
                                                        const unsigned short* __restrict__ wB, const unsigned short* __restrict__ woB,
                                                        unsigned char* __restrict__ out, unsigned char* ws) {
    extern __shared__ __attribute__((aligned(16))) unsigned char smem[];
    Ctx X;
    X.ws = __builtin_amdgcn_make_buffer_rsrc(ws, 0, WS_BYTES, 0x00020000);
    X.wsp = ws; X.lds = smem;
    X.lane = threadIdx.x & 63; X.hi = X.lane >> 5; X.u = __builtin_amdgcn_readfirstlane(threadIdx.x >> 6);
    { const int b = blockIdx.x, i = b >> 3; X.t = (b & 7) * 2 + (i >> 4); X.c = i & 15; X.cu = X.t * 16 + X.c; }
    X.t0 = X.t; X.c0 = X.c; X.u0 = X.u;
    const int rho = X.lane & 31;
    const unsigned long long gen = __hip_atomic_load((gu64*)ws, RLX_AGENT);

    const int ps = X.cu >> 3, pg = X.cu & 7;
    unsigned short we[4][8];
#pragma unroll
    for (int f = 0; f < 4; ++f) {
        const int mat = f >> 1, c2 = 2 * pg + (f & 1);
        if (X.u < 3) {
            int col;
            if (mat == 0) col = (X.u == 2) ? 1024 + 32 * c2 + rho : (rho >> 4) * 512 + 16 * (2 * c2 + X.u) + (rho & 15);
            else col = X.u * 512 + 32 * c2 + rho;
            const unsigned short* p = (mat ? wB : wA) + (32 * (ps >> 1) + 16 * X.hi + 8 * (ps & 1)) * 1536 + col;
#pragma unroll
            for (int j = 0; j < 8; ++j) we[f][j] = p[j * 1536];
        } else {
            const int col = 32 * c2 + 16 * ((rho >> 2) & 1) + (rho & 3) + 4 * (rho >> 3);
            const unsigned short* p = (mat ? woB : woA) + (16 * ps + 4 * X.hi) * 512 + col;
#pragma unroll
            for (int j = 0; j < 8; ++j) we[f][j] = p[((j & 3) + 8 * (j >> 2)) * 512];
        }
    }
    u32x4 xf[32];
    u32x4 xr[2];
    {
        const unsigned char* xp = (const unsigned char*)xin + (size_t)(X.t * 128 + X.u * 32 + rho) * 1024 + X.hi * 32;
#pragma unroll
        for (int s = 0; s < 32; ++s) xf[s] = *(const u32x4*)(xp + (s >> 1) * 64 + (s & 1) * 16);
        xr[0] = *(const u32x4*)(xp + X.c * 64); xr[1] = *(const u32x4*)(xp + X.c * 64 + 16);
    }
    CBAR();
    X.epoch = __builtin_amdgcn_readfirstlane((unsigned)gen) | ((unsigned long long)__builtin_amdgcn_readfirstlane((unsigned)(gen >> 32)) << 32);
    X.epoch += 1;
#pragma unroll
    for (int f = 0; f < 4; ++f) {
        const int mat = f >> 1, c2 = 2 * pg + (f & 1);
        u32x4 v;
#pragma unroll
        for (int i = 0; i < 4; ++i) v[i] = (unsigned)we[f][2 * i] | ((unsigned)we[f][2 * i + 1] << 16);
        if (X.u < 3) {
            stw(X, v, X.lane * 16, OFF_W + mat * SZ_W + (c2 * 96 + ps * 3 + X.u) * 1024);
            float s = 0.f;
#pragma unroll
            for (int i = 0; i < 4; ++i) s += bflo(v[i]) + bfhi(v[i]);
            s = xhalf_sum(s);
            if (X.lane < 32) __builtin_amdgcn_raw_buffer_store_b32(__float_as_uint(s), X.ws, X.lane * 4, OFF_CSP + mat * SZ_CSP + (c2 * 32 + ps) * 384 + X.u * 128, 16);
        } else {
            stw(X, v, X.lane * 16, OFF_WO + mat * SZ_WO + (c2 * 32 + ps) * 1024);
        }
    }
    publish(X, 0);
    float ra, rb;
    {
        float s1 = 0.f, s2 = 0.f;
        const bf16x2 one2 = __builtin_bit_cast(bf16x2, 0x3f803f80u);
#pragma unroll
        for (int s = 0; s < 32; ++s)
#pragma unroll
            for (int i = 0; i < 4; ++i) {
                const unsigned xw = xf[s][i];
                const bf16x2 v = __builtin_bit_cast(bf16x2, xw);
                s1 = __builtin_amdgcn_fdot2_f32_bf16(v, one2, s1, false);
                s2 = __builtin_amdgcn_fdot2_f32_bf16(v, v, s2, false);
            }
        finish_stats(X, xhalf_sum(s1), xhalf_sum(s2), ra, rb);
    }
    waitflags(X, 0, X.c >> 1, 8, 32);
    glds_copy(X, OFF_W + X.c * 98304, L_W, 24);
    glds_copy(X, OFF_WO + X.c * 32768, L_WO, 8);
    if (X.u < 3) {
#pragma unroll
        for (int mat = 0; mat < 2; ++mat) {
            float s = 0.f; unsigned pv[16];
#pragma unroll
            for (int i = 0; i < 16; ++i) pv[i] = __builtin_amdgcn_raw_buffer_load_b32(X.ws, (X.hi * 16 + i) * 384 + rho * 4, OFF_CSP + mat * SZ_CSP + X.c * 32 * 384 + X.u * 128, 16);
#pragma unroll
            for (int i = 0; i < 16; ++i) s += __uint_as_float(pv[i]);
            s = xhalf_sum(s);
            if (X.lane < 32) *(float*)(smem + L_CS + mat * 384 + X.u * 128 + X.lane * 4) = s;
        }
    }
    drain();
    bar();

    for (int it = 0;; ++it) {
        const int hb = it * 7;
        {
            fresh(X);
            f32x16 a0, a1, a2;
            gemm_qkv<true>(X, xf, a0, a1, a2);
            ln_fix_T(X, a0, L_CS, ra, rb); ln_fix_T(X, a1, L_CS + 128, ra, rb); ln_fix_N(X, a2, L_CS + 256);
            const unsigned tile = 4 * X.t + X.u;
            const unsigned so = OFF_KV + it * (4u << 20) + (tile * 32 + 2 * X.c) * 2048;
            const int lb = L_QKV + X.u * 6144;
#pragma unroll
            for (int hd = 0; hd < 2; ++hd) {
                f32x16& a = hd ? a1 : a0;
                float m = 0.f;
#pragma unroll
                for (int j = 0; j < 8; ++j) { a[j] *= QSCALE; m = fmaf(a[j], a[8 + j], m); }
                m = xhalf_sum(m);
                const u32x4 q = pack8(a, 0), k = pack8(a, 8);
                stw(X, k, X.lane * 16 + hd * 2048, so);
                *(u32x4*)(smem + lb + hd * 3072 + X.lane * 16) = q;
                *(u32x4*)(smem + lb + hd * 3072 + 1024 + X.lane * 16) = k;
                if (X.lane < 32) *(float*)(smem + L_MH + (X.u * 2 + hd) * 128 + X.lane * 4) = m;
            }
            {
                const int hbit = (X.lane >> 4) & 1, ch = (X.hi * 16 + (X.lane & 15)) * 16;
                const u32x4 v0 = pack8(a2, 0), v1 = pack8(a2, 8);
                stw(X, v0, hbit * 2048 + 1024 + ch, so); stw(X, v1, hbit * 2048 + 1536 + ch, so);
                *(u32x4*)(smem + lb + hbit * 3072 + 2048 + ch) = v0; *(u32x4*)(smem + lb + hbit * 3072 + 2560 + ch) = v1;
            }
            publish(X, hb + 1);
            fresh(X);
            if (X.t > 0) waitflags(X, hb + 1, (X.t - 1) * 16 + X.c, -16, X.t > 1 ? 2 : 1);
            if (X.u < 2) attention<0, 3>(X, it); else attention<1, 2>(X, it);
            glds_copy(X, OFF_W + SZ_W + X.c * 98304, L_W, 24);
            publish(X, hb + 2);
            waitflags(X, hb + 2, X.t * 16, 1, 16);
            fresh(X);
            ld32(X, xf, OFF_O + (2 * it) * (2u << 20) + (4 * X.t + X.u) * 32768);
            CBAR();
            f32x16 acc = zero16();
#pragma unroll
            for (int s = 0; s < 32; ++s) acc = mfma(lds128(X, L_WO + s * 1024 + X.lane * 16), xf[s], acc);
            outproj_finish(X, acc, xr, 2 * it, false, out);
            publish(X, hb + 3);
            glds_copy(X, OFF_WO + SZ_WO + X.c * 32768, L_WO, 8);
        }
        {
            fresh(X);
            waitflags(X, hb + 3, X.t * 16, 1, 16);
            drain();
            load_x(X, 2 * it, xf, ra, rb);
            f32x16 a0, a1, a2;
            gemm_qkv<false>(X, xf, a0, a1, a2);
            ln_fix_T(X, a0, L_CS + 384, ra, rb); ln_fix_N(X, a1, L_CS + 384 + 128); ln_fix_N(X, a2, L_CS + 384 + 256);
            const unsigned tile = 4 * X.t + X.u;
#pragma unroll
            for (int i = 0; i < 16; ++i) { a0[i] = phi(a0[i]); a1[i] = phi(a1[i]); }
            const u32x4 ones = {0x3f803f80u, 0x3f803f80u, 0x3f803f80u, 0x3f803f80u};
            const u32x4 k0 = pack8(a1, 0), k1 = pack8(a1, 8);
            f32x16 kv = mfma(k0, pack8(a2, 0), zero16()); kv = mfma(k1, pack8(a2, 8), kv);
            f32x16 zz = mfma(k0, ones, zero16()); zz = mfma(k1, ones, zz);
            const bool hbit = X.lane & 16;
            f32x4 e0, e1, z0, z1;
#pragma unroll
            for (int j = 0; j < 4; ++j) { e0[j] = hbit ? kv[8 + j] : kv[j]; e1[j] = hbit ? kv[12 + j] : kv[4 + j]; z0[j] = hbit ? zz[8 + j] : zz[j]; z1[j] = hbit ? zz[12 + j] : zz[4 + j]; }
            *(f32x4*)(smem + L_RED + X.u * 2176 + X.lane * 32) = e0; *(f32x4*)(smem + L_RED + X.u * 2176 + X.lane * 32 + 16) = e1;
            if ((X.lane & 15) == 0) { *(f32x4*)(smem + L_RED + X.u * 2176 + 2048 + (X.lane >> 4) * 32) = z0; *(f32x4*)(smem + L_RED + X.u * 2176 + 2048 + (X.lane >> 4) * 32 + 16) = z1; }
            stw(X, pack8(a0, 0), X.lane * 16, OFF_O + (2 * it + 1) * (2u << 20) + (tile * 32 + 2 * X.c) * 1024);
            stw(X, pack8(a0, 8), X.lane * 16 + 1024, OFF_O + (2 * it + 1) * (2u << 20) + (tile * 32 + 2 * X.c) * 1024);
            bar();
            fresh(X);
            const int tid = X.u * 64 + X.lane;
            if (it == 0) glds_copy(X, OFF_W + X.c * 98304, L_W, 24);
            if (tid < 136) {
                const int o = L_RED + tid * 16;
                const f32x4 s = (lds128f(X, o) + lds128f(X, o + 2176)) + (lds128f(X, o + 2 * 2176) + lds128f(X, o + 3 * 2176));
                stw(X, __builtin_bit_cast(u32x4, s), tid * 16, OFF_KVP + it * SZ_KVP + (X.c * 16 + X.t) * 2304);
            }
            publish(X, hb + 4);
            if (X.u == 0 || (X.u == 1 && X.t == X.c)) {
                waitflags(X, hb + 4, X.c, 16, 16);
                const int rho = X.lane & 31;
                const unsigned fo = (X.u ? 2048u : (unsigned)X.t * 128u) + rho * 4;
                unsigned pv[8];
#pragma unroll
                for (int i = 0; i < 8; ++i) pv[i] = __builtin_amdgcn_raw_buffer_load_b32(X.ws, (X.hi * 8 + i) * 2304 + fo, OFF_KVP + it * SZ_KVP + X.c * 16 * 2304, 16);
                float s = 0.f;
#pragma unroll
                for (int i = 0; i < 8; ++i) s += __uint_as_float(pv[i]);
                s = xhalf_sum(s);
                const float nx = __builtin_amdgcn_mov_dpp(s, 0xB1, 0xF, 0xF, true);
                unsigned dst;
                if (X.u == 0) dst = (2 * X.c + ((X.t >> 2) & 1)) * 640 + ((X.t >> 3) * 16 + ((4 * X.t) & 15)) * 16 + (rho >> 1) * 4;
                else dst = (2 * X.c + ((rho >> 3) & 1)) * 640 + 512 + (rho >> 4) * 16 + ((rho & 7) >> 1) * 4;
                if (X.lane < 32 && !(X.lane & 1)) __builtin_amdgcn_raw_buffer_store_b32(pk2(s, nx), X.ws, dst, OFF_KVF + it * SZ_KVF, 16);
            }
            publish(X, hb + 5);
            waitflags(X, hb + 4, X.t * 16, 1, 16);
            fresh(X);
            ld32(X, xf, OFF_O + (2 * it + 1) * (2u << 20) + (4 * X.t + X.u) * 32768);
            CBAR();
            waitflags(X, hb + 5, X.u * 64, 1, 64);
            bar();
            {
                u32x4 g[5];
#pragma unroll
                for (int i = 0; i < 4; ++i) { const int id = i * 256 + X.u * 64 + X.lane; g[i] = ldw(X, (id >> 5) * 640 + (id & 31) * 16, OFF_KVF + it * SZ_KVF); }
                if (X.u == 0) g[4] = ldw(X, (X.lane >> 1) * 640 + 512 + (X.lane & 1) * 16, OFF_KVF + it * SZ_KVF);
#pragma unroll
                for (int i = 0; i < 4; ++i) { const int id = i * 256 + X.u * 64 + X.lane; *(u32x4*)(smem + L_KVF + (id >> 5) * 544 + (id & 31) * 16) = g[i]; }
                if (X.u == 0) *(u32x4*)(smem + L_KVF + (X.lane >> 1) * 544 + 512 + (X.lane & 1) * 16) = g[4];
            }
            bar();
            f32x16 acc = zero16();
            const int ko = L_KVF + ((X.lane & 16) ? 512 + X.hi * 16 : (X.hi * 16 + (X.lane & 15)) * 16);
#pragma unroll
            for (int h = 0; h < 32; ++h) {
                const f32x16 nd = mfma(lds128(X, ko + h * 544), xf[h], zero16());
                const float inv = __builtin_amdgcn_rcpf(nd[8] + 1e-6f);
                const u32x4 o = {pk2(nd[0] * inv, nd[1] * inv), pk2(nd[2] * inv, nd[3] * inv), pk2(nd[4] * inv, nd[5] * inv), pk2(nd[6] * inv, nd[7] * inv)};
                acc = mfma(lds128(X, L_WO + h * 1024 + X.lane * 16), o, acc);
            }
            outproj_finish(X, acc, xr, 1, it == 1, out);
            if (it == 1) break;
            publish(X, 7);
            glds_copy(X, OFF_WO + X.c * 32768, L_WO, 8);
            drain();
            fresh(X);
            waitflags(X, 7, X.t * 16, 1, 16);
            load_x(X, 1, xf, ra, rb);
        }
    }
    if (blockIdx.x == 0 && threadIdx.x == 0) __hip_atomic_store((gu64*)ws, X.epoch, RLX_AGENT);
}

extern "C" void kernel_launch(void* const* d_in, const int* in_sizes, int n_in, void* d_out, int out_size, void* d_ws, size_t ws_size, hipStream_t stream) {
    (void)in_sizes; (void)n_in; (void)out_size; (void)ws_size;
    (void)hipFuncSetAttribute((const void*)fused_kernel, hipFuncAttributeMaxDynamicSharedMemorySize, LDS_BYTES);
    fused_kernel<<<256, 256, LDS_BYTES, stream>>>((const unsigned short*)d_in[0], (const unsigned short*)d_in[1], (const unsigned short*)d_in[2],
                                                  (const unsigned short*)d_in[4], (const unsigned short*)d_in[5], (unsigned char*)d_out, (unsigned char*)d_ws);
}
```

```cpp
#include <hip/hip_runtime.h>
#include <hip/hip_bf16.h>
#include <type_traits>

typedef __bf16 bf16x8 __attribute__((ext_vector_type(8)));
typedef __bf16 bf16x2 __attribute__((ext_vector_type(2)));
typedef float f32x16 __attribute__((ext_vector_type(16)));
typedef float f32x4 __attribute__((ext_vector_type(4)));
typedef float f32x2 __attribute__((ext_vector_type(2)));
typedef unsigned u32x4 __attribute__((ext_vector_type(4)));
typedef unsigned u32x2 __attribute__((ext_vector_type(2)));
typedef short s16x4 __attribute__((ext_vector_type(4)));
#define GAS __attribute__((address_space(1)))
#define LAS __attribute__((address_space(3)))
typedef GAS unsigned long long gu64;
#define DI __device__ __forceinline__
#define RLX_AGENT __ATOMIC_RELAXED, __HIP_MEMORY_SCOPE_AGENT
#define CBAR() asm volatile("" ::: "memory")
#define SBAR() __builtin_amdgcn_sched_barrier(0)
#define PIN(x) asm volatile("" : "+v"(x))
#define UNLIKELY(x) __builtin_expect(!!(x), 0)
template <int N> using IC = std::integral_constant<int, N>;

constexpr unsigned OFF_START = 4096;
constexpr unsigned OFF_WFLAG = 8192;
constexpr unsigned OFF_ONES = 512u << 10;
constexpr unsigned OFF_W = 1u << 20;
constexpr unsigned SZ_W = 1572864;
constexpr unsigned OFF_WO = OFF_W + 2 * SZ_W;
constexpr unsigned SZ_WO = 524288;
constexpr unsigned OFF_X = 6u << 20;
constexpr unsigned OFF_KV = 13u << 20;
constexpr unsigned OFF_O = 21u << 20;
constexpr unsigned OFF_KVP = 29u << 20;
constexpr unsigned SZ_KVP = 16 * 16 * 2304;
constexpr unsigned OFF_KVF = 31u << 20;
constexpr unsigned SZ_KVF = 32768;
constexpr unsigned OFF_ST = 32u << 20;
constexpr unsigned SZ_ST = 524288;
constexpr unsigned WS_BYTES = 34u << 20;
constexpr unsigned MB2 = 2u << 20;
constexpr unsigned OOB = 0x80000000u;
constexpr unsigned MARK = 0xffffffffu;

constexpr unsigned L_W = 0, L_WO = 98304, L_S = 131072;
constexpr unsigned L_XI = 31744, L_RT = L_WO;
constexpr unsigned L_QKV = L_S, L_MH = L_S + 24576, L_STAT = L_S + 25600, L_CSF = L_S + 26112;
constexpr unsigned L_RED = L_S, L_KVF = L_S, L_EX = L_S;
constexpr unsigned L_ONES = L_CSF + 6144;
constexpr unsigned L_CNT = L_ONES + 32;
constexpr unsigned L_DBG = L_ONES + 48;
constexpr unsigned LDS_BYTES = L_ONES + 512;

constexpr float QSCALE = 0.25f * 1.4426950408889634f;
constexpr float LOG2E = 1.4426950408889634f;
constexpr unsigned SPIN_CAP = 1u << 18;
constexpr unsigned ONE2 = 0x3f803f80u;

#define DELAY_O 12
#define DELAY_X 12
#define DELAY_KVP 8
#define DELAY_KVF 8
#define DELAY_W 4
#define DELAY_PQ 3
#define DELAY_WO 36
#define DELAY_MK 0
#define MAPPING 1
#define WA_EARLY 16
#define WA_DEPTH 4
#define ATT_PRIO 1
#define MARK_EARLY 0
#define KVP_BACKOFF 3
#define PD 8
#define TOUCH 0
#define WS_SHIFT (100u << 20)
#define XD 8
#define XR 10
#define OD 6
#define HALO0 0
#define PQ_SPLIT 8
#define STAMP(i) do {} while (0)
#define STAMPU(i, u) do {} while (0)
#define COUNT(i) do {} while (0)

struct Ctx {
    __amdgpu_buffer_rsrc_t ws;
    unsigned char* wsp;
    unsigned e0, e1;
    int t, c, u, g, lane, hi;
    unsigned l16;
    int t0, c0, u0, g0, x0;
};

DI f32x16 mfma(u32x4 a, u32x4 b, f32x16 c) {
    return __builtin_amdgcn_mfma_f32_32x32x16_bf16(__builtin_bit_cast(bf16x8, a), __builtin_bit_cast(bf16x8, b), c, 0, 0, 0);
}
DI unsigned pk2(float a, float b) { bf16x2 v = {(__bf16)a, (__bf16)b}; return __builtin_bit_cast(unsigned, v); }
DI unsigned pk2(f32x2 a) { return pk2(a[0], a[1]); }
DI float bflo(unsigned x) { return __uint_as_float(x << 16); }
DI float bfhi(unsigned x) { return __uint_as_float(x & 0xffff0000u); }
DI float xhalf_sum2(float a, float b) {
    auto r = __builtin_amdgcn_permlane32_swap(__float_as_uint(a), __float_as_uint(b), false, false); return __uint_as_float(r[0]) + __uint_as_float(r[1]);
}
DI float xhalf_sum(float x) { return xhalf_sum2(x, x); }
DI float xhalf_max(float x) { auto r = __builtin_amdgcn_permlane32_swap(__float_as_uint(x), __float_as_uint(x), false, false); return fmaxf(__uint_as_float(r[0]), __uint_as_float(r[1])); }
DI u32x4 ldw(const Ctx& X, unsigned voff, unsigned soff) { return __builtin_amdgcn_raw_buffer_load_b128(X.ws, voff, soff, 16); }
DI void stw(const Ctx& X, u32x4 v, unsigned voff, unsigned soff) {
    __builtin_amdgcn_raw_buffer_store_b128(v, X.ws, voff, soff, 16);
    asm volatile("s_nop 1" :: "v"(v));
}
DI void stw32(const Ctx& X, unsigned v, unsigned voff, unsigned soff) { __builtin_amdgcn_raw_buffer_store_b32(v, X.ws, voff, soff, 16); }
DI u32x4 lds128(unsigned a) { return *(const LAS u32x4*)(size_t)a; }
DI f32x4 lds128f(unsigned a) { return *(const LAS f32x4*)(size_t)a; }
DI float lds32f(unsigned a) { return *(const LAS float*)(size_t)a; }
DI void sts128(unsigned a, u32x4 v) { *(LAS u32x4*)(size_t)a = v; }
DI void sts128f(unsigned a, f32x4 v) { *(LAS f32x4*)(size_t)a = v; }
DI void sts32f(unsigned a, float v) { *(LAS float*)(size_t)a = v; }
DI void bar() { asm volatile("s_waitcnt lgkmcnt(0)\n\ts_barrier" ::: "memory"); }
template <int N> DI void vmwait() { asm volatile("s_waitcnt vmcnt(%0)" :: "n"(N) : "memory"); }
DI void vmdrain() { CBAR(); __builtin_amdgcn_s_waitcnt(0x0F70); CBAR(); }
template <int N> DI void lgkwait() { __builtin_amdgcn_s_waitcnt(0xC07F | (N << 8)); }
DI f32x16 zero16() { f32x16 z; _Pragma("unroll") for (int i = 0; i < 16; ++i) z[i] = 0.f; return z; }
DI u32x4 splat4(unsigned v) { u32x4 r = {v, v, v, v}; return r; }
DI unsigned umax3(unsigned a, unsigned b, unsigned c) { return max(max(a, b), c); }
DI unsigned umax4(u32x4 a) { return max(umax3(a[0], a[1], a[2]), a[3]); }
DI unsigned umax8(u32x4 a, u32x4 b) { return max(umax3(umax3(umax3(a[0], a[1], a[2]), a[3], b[0]), b[1], b[2]), b[3]); }
DI bool marked(unsigned m) { return __builtin_amdgcn_ballot_w64(m == MARK) != 0; }
DI float opaque(float x) { PIN(x); return x; }
DI unsigned opaque(unsigned x) { PIN(x); return x; }
DI unsigned sopaque(unsigned x) { asm volatile("" : "+s"(x)); return x; }
DI f32x2 pair(const f32x16& a, int i) { f32x2 r = {a[i], a[i + 1]}; return r; }
template <int I, int N, class F> DI void static_for(F&& f) { if constexpr (I < N) { f(IC<I>{}); static_for<I + 1, N>(f); } }

DI void fresh(Ctx& X) {
    X.lane = __builtin_amdgcn_mbcnt_hi(~0u, __builtin_amdgcn_mbcnt_lo(~0u, 0u));
    PIN(X.lane);
    X.t = X.t0; X.c = X.c0; X.u = X.u0; X.g = X.g0;
    asm volatile("" : "+s"(X.t), "+s"(X.c), "+s"(X.u), "+s"(X.g));
    X.hi = X.lane >> 5; X.l16 = X.lane * 16;
}
template <int IMM, int AUX> DI void dma1(__amdgpu_buffer_rsrc_t r, unsigned voff, unsigned soff, unsigned lds) {
    __builtin_amdgcn_raw_ptr_buffer_load_lds(r, (LAS void*)(size_t)lds, 16, voff, soff, IMM, AUX);
}
template <int A, int B> DI void dma_lin(const Ctx& X, unsigned goff, unsigned loff) {
    static_for<A, B>([&](auto I) { constexpr int i = I; dma1<(i & 3) * 1024, 16>(X.ws, X.l16, goff + (i >> 2) * 4096, loff + (i >> 2) * 4096); });
}
DI void flag_store(const Ctx& X, unsigned off) {
    if (X.lane == 0) __hip_atomic_store((gu64*)(X.wsp + off), (unsigned long long)X.e0 | ((unsigned long long)X.e1 << 32), RLX_AGENT);
    CBAR();
}

template <int A, int B, int N> DI void ld_range(const Ctx& X, u32x4 (&xf)[N], unsigned base) {
#pragma unroll
    for (int s = A; s < B; ++s) xf[s] = ldw(X, X.l16 + (s & 3) * 1024, base + (s >> 2) * 4096);
}
template <int P, int END, int cnt, int N> DI void verify_pair(const Ctx& X, u32x4 (&xf)[N], unsigned base, unsigned m) {
    if (UNLIKELY(marked(m))) {
        COUNT(cnt);
        unsigned spins = 0;
#pragma nounroll
        for (; spins < SPIN_CAP; ++spins) {
            CBAR();
            ld_range<2 * P, 2 * P + 2>(X, xf, base);
            if (!marked(umax8(xf[2 * P], xf[2 * P + 1]))) break;
            __builtin_amdgcn_s_sleep(2);
        }
        CBAR();
        if (spins) ld_range<2 * P + 2, END>(X, xf, base);
        vmdrain();
    }
}

DI unsigned tile_of(const Ctx& X) { return 4 * X.t + X.u; }
DI unsigned kv_so(const Ctx& X, int la) { return OFF_KV + la * (2 * MB2) + tile_of(X) * 65536 + X.c * 4096; }
DI unsigned fr_so(const Ctx& X, unsigned buf) { return buf + tile_of(X) * 32768 + X.c * 2048; }
DI unsigned st_so(const Ctx& X, int li) { return OFF_ST + li * SZ_ST + tile_of(X) * 8192 + X.c * 512; }
DI unsigned kvp_so(const Ctx& X, int lb) { return OFF_KVP + lb * SZ_KVP + (X.c * 16 + X.t) * 2304; }
DI unsigned kvf_vo(const Ctx& X, int z) {
    const int j = X.lane >> 4, p = X.lane & 15;
    const unsigned o = z ? (2 * X.c + (j & 1)) * 544 + 512 + (j >> 1) * 16 : (2 * X.c + ((X.t >> 2) & 1)) * 544 + ((X.t >> 3) * 16 + ((4 * X.t) & 15) + j) * 16;
    return p < 8 ? p * SZ_KVF + o : OOB;
}
DI void mark2(const Ctx& X, unsigned so) { const u32x4 m = splat4(MARK); stw(X, m, X.l16, so); stw(X, m, X.l16 + 1024, so); }
DI void mark_x(const Ctx& X, int li) {
    mark2(X, fr_so(X, OFF_X + li * MB2));
    __builtin_amdgcn_raw_buffer_store_b64(u32x2{MARK, MARK}, X.ws, X.lane * 8, st_so(X, li), 16);
}

DI void stats_to_frag(const Ctx& X, float s1, float s2, float& ra, u32x4& mf) {
    const float mean = s1 * (1.f / 512.f);
    const float var = fmaxf(s2 * (1.f / 512.f) - mean * mean, 0.f);
    ra = __builtin_amdgcn_rsqf(var + 1e-5f);
    const unsigned h = pk2(-mean, -mean);
    const float lo = -mean - bflo(h);
    mf[0] = h; mf[1] = pk2(lo, lo); mf[2] = 0; mf[3] = 0;
    sts32f(L_STAT + X.u * 128 + (X.lane & 31) * 4, ra);
}
DI void colsum_add(u32x4 v, float& s0, float& s1) {
#pragma unroll
    for (int i = 0; i < 4; ++i) { s0 += bflo(v[i]); s1 += bfhi(v[i]); }
    PIN(s0); PIN(s1);
}
DI void colsum_frag(const Ctx& X, int mat, int tile, float s) {
    const unsigned hh = pk2(s, s) & 0xffffu;
    const unsigned w = X.hi ? 0u : (hh | (pk2(s - bflo(hh), 0.f) << 16));
    sts128(L_CSF + mat * 3072 + tile * 1024 + X.l16, u32x4{w, w, 0u, 0u});
}
DI u32x4 tr_frag(unsigned a, unsigned step) {
    const u32x2 l2 = __builtin_bit_cast(u32x2, __builtin_amdgcn_ds_read_tr16_b64_v4i16((LAS s16x4*)(size_t)a));
    const u32x2 h2 = __builtin_bit_cast(u32x2, __builtin_amdgcn_ds_read_tr16_b64_v4i16((LAS s16x4*)(size_t)(a + step)));
    return u32x4{l2[0], l2[1], h2[0], h2[1]};
}
DI unsigned tr_lane64(const Ctx& X) { return (8 * X.hi + ((X.lane >> 2) & 3)) * 64 + ((X.lane >> 4) & 1) * 32 + (X.lane & 3) * 8; }

template <bool ALL> DI void wait_progress(u32x4 cf, unsigned need) {
#pragma nounroll
    for (unsigned spins = 0; spins < SPIN_CAP * 16 && UNLIKELY(__builtin_amdgcn_readfirstlane(ALL ? min(min(cf[0], cf[1]), cf[2]) : cf[0]) < need); ++spins) {
        __builtin_amdgcn_s_sleep(1);
        cf = *(volatile LAS u32x4*)(size_t)L_CNT;
    }
    CBAR();
}
DI void set_progress(const Ctx& X, unsigned v) { CBAR(); *(volatile LAS unsigned*)(size_t)(L_CNT + X.u * 4) = v; CBAR(); }
constexpr int xissued(int s) { int n = XD + (s < XR ? 2 * s : XR + s); return n < 32 ? n : 32; }
template <int MODE>
DI void gemm_qkv(Ctx& X, u32x4 (&xf)[32], int li, f32x16& a0, f32x16& a1, f32x16& a2, float& ra, f32x4 (&rn)[4], u32x4 mf) {
    constexpr bool LA = MODE != 2;
    const unsigned xbase = OFF_X + li * MB2 + tile_of(X) * 32768;
    u32x4 st[8];
    const unsigned stv = X.hi * 4096 + (X.lane & 31) * 16, sts = OFF_ST + li * SZ_ST + tile_of(X) * 8192;
    if constexpr (MODE != 0) { ld_range<0, XD>(X, xf, xbase); CBAR(); }
    a0 = zero16(); a1 = zero16(); a2 = zero16();
    u32x4 w[3][3];
    float s1 = 0.f, s2 = 0.f; unsigned sv = 0; u32x4 cf;
    const unsigned o = L_W + (MODE == 0 ? opaque(tr_lane64(X)) : X.l16), o2 = opaque(o + 49152), oc = opaque(L_CSF + (LA ? 0 : 3072) + X.l16);
    const unsigned orn = opaque(L_STAT + X.u * 128 + X.hi * 16);
#pragma unroll
    for (int s = 0; s < 2; ++s)
#pragma unroll
        for (int i = 0; i < 3; ++i) w[s][i] = MODE == 0 ? tr_frag(o + s * 3072 + i * 1024, 256) : lds128(o + s * 3072 + i * 1024);
    unsigned vm = 0;
    if constexpr (MODE != 0) verify_pair<0, XD, MODE>(X, xf, xbase, umax8(xf[0], xf[1]));
    static_for<0, 33>([&](auto S) {
        constexpr int s = S;
        auto wread = [&](int i) {
            if constexpr (s + 2 < 32) {
                const unsigned a = (s + 2 < 16 ? o + (s + 2) * 3072 : o2 + (s - 14) * 3072) + i * 1024;
                w[(s + 2) % 3][i] = MODE == 0 ? tr_frag(a, 256) : lds128(a);
            }
            else if constexpr (s == 30) w[(s + 2) % 3][i] = lds128(oc + i * 1024);
            else if constexpr (s == 31) { rn[i] = lds128f(orn + i * 32); if (i == 2) rn[3] = lds128f(orn + 96); }
        };
        constexpr unsigned need = MODE == 0 ? (s == 30 ? 33 : s + 6) : 34;
        if constexpr ((MODE == 0 && (s & 3) == 0) || (MODE == 2 && s == 28)) cf = *(volatile LAS u32x4*)(size_t)L_CNT;
        if constexpr ((MODE == 0 && (s & 3) == 2) || (MODE == 2 && s == 30)) wait_progress<s == 30>(cf, need);
        if constexpr (false) {}
        else if constexpr (MODE == 0 && s < 31) lgkwait<6>();
        else if constexpr (s < 32) lgkwait<3>(); else lgkwait<4>();
        SBAR();
        const u32x4 xv = s < 32 ? xf[s & 31] : mf;
        a0 = mfma(w[s % 3][0], xv, a0);
        PIN(a0);
        wread(0);
        if constexpr (MODE != 0 && s < 30) { vm = (s & 1) ? umax3(umax3(vm, xf[s + 2][0], xf[s + 2][1]), xf[s + 2][2], xf[s + 2][3]) : umax4(xf[s + 2]); PIN(vm); }
        SBAR();
        if (LA) a1 = mfma(w[s % 3][1], xv, a1); else a1 = mfma(xv, w[s % 3][1], a1);
        PIN(a1);
        wread(1);
        if constexpr (MODE != 0) ld_range<xissued(s), xissued(s + 1)>(X, xf, xbase);
        if constexpr (MODE != 0 && s >= 14 && s < 22) st[s - 14] = ldw(X, stv + (s - 14) * 512, sts);
        if constexpr (MODE != 0 && s >= 26 && s < 30) {
            constexpr int k = 2 * (s - 26);
            sv = umax3(sv, umax4(st[k]), umax4(st[k + 1]));
            s1 = (s1 + __uint_as_float(st[k][0])) + (__uint_as_float(st[k][2]) + opaque(__uint_as_float(st[k + 1][0]) + __uint_as_float(st[k + 1][2])));
            s2 = (s2 + __uint_as_float(st[k][1])) + (__uint_as_float(st[k][3]) + opaque(__uint_as_float(st[k + 1][1]) + __uint_as_float(st[k + 1][3])));
            PIN(s1); PIN(s2); PIN(sv);
        }
        CBAR(); SBAR();
        a2 = mfma(xv, w[s % 3][2], a2);
        PIN(a2);
        wread(2);
        if constexpr (MODE != 0 && s < 30 && (s & 1)) verify_pair<(s + 1) / 2, xissued(s + 1), MODE>(X, xf, xbase, vm);
        if constexpr (MODE != 0 && s == 30) {
            if (UNLIKELY(marked(sv))) {
                COUNT(3);
#pragma nounroll
                for (unsigned spins = 0; spins < SPIN_CAP; ++spins) {
                    CBAR();
#pragma unroll
                    for (int i = 0; i < 8; ++i) st[i] = ldw(X, stv + i * 512, sts);
                    if (!marked(max(umax3(umax8(st[0], st[1]), umax8(st[2], st[3]), umax8(st[4], st[5])), umax8(st[6], st[7])))) break;
                }
                vmdrain();
                s1 = 0.f; s2 = 0.f;
#pragma unroll
                for (int k = 0; k < 8; k += 2) {
                    s1 = (s1 + __uint_as_float(st[k][0])) + (__uint_as_float(st[k][2]) + (__uint_as_float(st[k + 1][0]) + __uint_as_float(st[k + 1][2])));
                    s2 = (s2 + __uint_as_float(st[k][1])) + (__uint_as_float(st[k][3]) + (__uint_as_float(st[k + 1][1]) + __uint_as_float(st[k + 1][3])));
                }
            }
            stats_to_frag(X, xhalf_sum(s1), xhalf_sum(s2), ra, mf);
        }
        SBAR();
    });
}
DI u32x4 pack8(const f32x16& a, int b) { u32x4 r = {pk2(a[b], a[b + 1]), pk2(a[b + 2], a[b + 3]), pk2(a[b + 4], a[b + 5]), pk2(a[b + 6], a[b + 7])}; return r; }
DI u32x4 pack4x2(f32x2 p0, f32x2 p1, f32x2 p2, f32x2 p3) {
    asm volatile("" : "+v"(p0), "+v"(p1), "+v"(p2), "+v"(p3));
    u32x4 r = {pk2(p0), pk2(p1), pk2(p2), pk2(p3)}; return r;
}
DI u32x4 pack8s(const f32x16& a, int b, float s) { return pack4x2(pair(a, b) * s, pair(a, b + 2) * s, pair(a, b + 4) * s, pair(a, b + 6) * s); }
DI u32x4 pack8m(const f32x16& a, float s) {
    float t[8];
#pragma unroll
    for (int i = 0; i < 8; ++i) t[i] = opaque(a[i] * s);
    u32x4 r = {pk2(t[0], t[1]), pk2(t[2], t[3]), pk2(t[4], t[5]), pk2(t[6], t[7])}; return r;
}
DI f32x2 rn2(const f32x4 (&rn)[4], int i) { f32x2 r = {rn[i >> 2][i & 3], rn[i >> 2][(i & 3) + 1]}; return r; }
DI u32x4 pack8r(const f32x16& a, int b, const f32x4 (&rn)[4]) {
    return pack4x2(pair(a, b) * rn2(rn, b), pair(a, b + 2) * rn2(rn, b + 2), pair(a, b + 4) * rn2(rn, b + 4), pair(a, b + 6) * rn2(rn, b + 6));
}

constexpr unsigned long long diag_mask(int r) {
    unsigned long long m = 0;
    for (int l = 0; l < 64; ++l) if ((r & 3) + 8 * (r >> 2) + 4 * (l >> 5) <= (l & 31)) m |= 1ull << l;
    return m;
}
struct KVt { u32x4 k, v0, v1; };
template <int I> DI unsigned pmask() {
    return (__builtin_amdgcn_inverse_ballot_w64(diag_mask(2 * I)) ? 0xffffu : 0u) | (__builtin_amdgcn_inverse_ballot_w64(diag_mask(2 * I + 1)) ? 0xffff0000u : 0u);
}
struct KVaddr { unsigned kb, vb, vs; int t4; };
DI KVaddr kv_addr(const Ctx& X, int la) {
    KVaddr a; a.t4 = 4 * X.t;
    a.kb = OFF_KV + la * (2 * MB2) + X.t * 262144 + (2 * X.c + (X.u & 1)) * 2048;
    const bool on = X.lane & 16;
    a.vb = opaque(on ? OFF_ONES + (X.t * 16 + X.c) * 1024 : a.kb + 1024 + (X.hi * 16 + (X.lane & 15)) * 16);
    a.vs = opaque(on ? 0u : 65536u);
    return a;
}
template <int PART> DI void kv_load(const Ctx& X, const KVaddr& a, KVt& r, int e) {
    const bool ok = a.t4 + e >= 0;
    if constexpr (PART & 1) r.k = ldw(X, X.l16, ok ? a.kb + e * 65536 : OOB);
    if constexpr (PART & 2) { const unsigned vo = a.vb + a.vs * e, so = ok ? 0u : OOB; r.v0 = ldw(X, vo, so); r.v1 = ldw(X, vo + 512, so); }
}
DI void att_slow(const Ctx& X, const KVaddr& a, int q0, u32x4 q, f32x16& o) {
    const int qi = X.lane & 31;
    float m = -INFINITY;
    o = zero16();
#pragma nounroll
    for (int pass = 0; pass < 2; ++pass) {
#pragma nounroll
        for (int d = 0; d <= 8 && a.t4 + q0 - d >= 0; ++d) {
            KVt kv;
#pragma nounroll
            for (unsigned spins = 0; spins < SPIN_CAP; ++spins) {
                CBAR();
                kv_load<3>(X, a, kv, q0 - d);
                if (!marked(max(umax8(kv.k, kv.v0), umax4(kv.v1)))) break;
            }
            f32x16 s = mfma(kv.k, q, zero16());
            float mm = -INFINITY;
#pragma unroll
            for (int r = 0; r < 16; ++r) {
                const int dist = qi - ((r & 3) + 8 * (r >> 2) + 4 * X.hi) + 32 * d;
                s[r] = (dist >= 0 && dist < 256) ? s[r] : -INFINITY;
                mm = fmaxf(mm, s[r]);
            }
            if (pass == 0) m = fmaxf(m, mm);
            else {
#pragma unroll
                for (int r = 0; r < 16; ++r) s[r] = __builtin_amdgcn_exp2f(s[r] - m);
                o = mfma(kv.v0, pack8(s, 0), o); o = mfma(kv.v1, pack8(s, 8), o);
            }
        }
        m = xhalf_max(m);
    }
}
struct Item { int unit, e, slot; bool merged; };
constexpr Item ITEMS[4][8] = {
    {{1, 0, 4, 0}, {1, 1, 5, 0}, {0, -1, 0, 0}, {1, -1, 0, 0}, {0, -3, 1, 0}, {1, -3, 1, 0}, {0, -6, 2, 0}, {0, -8, 3, 1}},
    {{1, 2, 4, 0}, {0, -2, 0, 0}, {1, -2, 0, 0}, {0, -4, 1, 0}, {1, -4, 1, 0}, {0, -5, 2, 0}, {1, -5, 2, 1}, {0, -7, 3, 0}},
    {{1, 0, 4, 0}, {0, 0, 4, 0}, {0, -1, 0, 0}, {1, -1, 0, 0}, {0, -3, 1, 0}, {1, -3, 1, 0}, {0, -6, 2, 0}, {1, -6, 2, 1}},
    {{1, 1, 4, 0}, {0, -2, 0, 0}, {1, -2, 0, 0}, {0, -4, 1, 0}, {1, -4, 1, 0}, {0, -5, 2, 0}, {1, -5, 2, 0}, {0, -7, 3, 1}}};
constexpr int slot_e(int v, int slot) { for (int i = 0; i < 8; ++i) if (ITEMS[v][i].slot == slot) return ITEMS[v][i].e; return 99; }
constexpr int merged_unit(int v) { for (int i = 0; i < 8; ++i) if (ITEMS[v][i].merged) return ITEMS[v][i].unit; return 0; }

template <int V>
DI void attention(Ctx& X, int la, const KVaddr& ka, u32x4& qk_, f32x16& res) {
    constexpr int QA = V >> 1, QB = 3 - QA, G = V & 1;
    constexpr int NOWN = G ? 1 : 2;
    u32x4 q[2]; f32x16 o[2];
    KVt kv[7];
    const unsigned qb = L_QKV + (X.u & 1) * 3072 + X.l16;
    const unsigned vb = (X.lane & 16) ? L_ONES : L_QKV + (X.u & 1) * 3072 + 2048 + (X.hi * 16 + (X.lane & 15)) * 32;
    const unsigned vs = (X.lane & 16) ? 0u : 6144u;
    const unsigned mh = L_MH + (X.u & 1) * 128 + (X.lane & 31) * 4;
    unsigned va[3] = {vb + vs * slot_e(V, 4), vb + vs * (slot_e(V, 5) != 99 ? slot_e(V, 5) : 0), vb + vs * (merged_unit(V) ? QB : QA)};
    unsigned ks[4], vo[4], vz[4];
    static_for<0, 4>([&](auto J) {
        constexpr int j = J, e = slot_e(V, j) != 99 ? slot_e(V, j) : -1;
        const bool ok = ka.t4 + e >= 0;
        ks[j] = sopaque(ok ? ka.kb + e * 65536 : OOB); vz[j] = sopaque(ok ? 0u : OOB); vo[j] = opaque(ka.vb + ka.vs * e);
    });
    const unsigned wg = sopaque(OFF_W + SZ_W + X.c * 98304 + (X.g * 4 + X.u) * 12288), wl = sopaque(L_W + (X.g * 4 + X.u) * 12288);
    const unsigned wg0 = sopaque(la == 0 ? OFF_WO + X.c * 32768 + (X.g * 4 + X.u) * 4096 : wg + 8192), wl0 = sopaque(la == 0 ? L_WO + (X.g * 4 + X.u) * 4096 : wl + 8192);
    auto wdma = [&](auto K) { constexpr int k = K; if constexpr (k < 4) dma_lin<k, k + 1>(X, wg0, wl0); else dma_lin<k - 4, k - 3>(X, wg, wl); };
    const u32x4 one0 = {opaque(X.hi ? 0u : 0x3f80u), 0u, 0u, 0u};
    asm volatile("" : "+v"(va[0]), "+v"(va[1]), "+v"(va[2]));
    if (G) __builtin_amdgcn_s_setprio(ATT_PRIO);
    bar();
    STAMP(11 + la * 24);
    auto own = [&](int slot, int e) { kv[slot].k = lds128(qb + e * 6144 + 1024); kv[slot].v0 = lds128(va[slot - 4]); kv[slot].v1 = lds128(va[slot - 4] + 16); };
    auto halo = [&](int j) { kv[j].k = ldw(X, X.l16, ks[j]); kv[j].v0 = ldw(X, vo[j], vz[j]); kv[j].v1 = ldw(X, vo[j] + 512, vz[j]); };
    const unsigned mb = __float_as_uint(lds32f(mh + QB * 256)), ma = __float_as_uint(lds32f(mh + QA * 256));
    q[1] = lds128(qb + QB * 6144);
    own(4, slot_e(V, 4));
    q[0] = lds128(qb + QA * 6144);
    if constexpr (slot_e(V, 5) != 99) own(5, slot_e(V, 5));
    own(6, merged_unit(V) ? QB : QA);
    SBAR();
    f32x16 nm[2], s[2], sx;
    o[0] = zero16(); o[1] = zero16();
    nm[1] = mfma(one0, u32x4{mb, 0u, 0u, 0u}, zero16()); nm[0] = mfma(one0, u32x4{ma, 0u, 0u, 0u}, zero16());
    u32x4 p0, p1;
    auto qk = [&](auto N) {
        constexpr int n = N; constexpr Item it = ITEMS[V][n];
        if constexpr (it.e < 0 && ITEMS[V][n > 0 ? n - 1 : 0].e != it.e) {
            if (UNLIKELY(marked(umax3(kv[it.slot].k[0], kv[it.slot].v0[0], kv[it.slot].v1[0])))) {
                COUNT(4);
#pragma nounroll
                for (unsigned spins = 0; spins < SPIN_CAP; ++spins) {
                    CBAR();
                    halo(it.slot);
                    if (!marked(umax3(kv[it.slot].k[0], kv[it.slot].v0[0], kv[it.slot].v1[0]))) break;
                    __builtin_amdgcn_s_sleep(2);
                }
                CBAR();
                static_for<it.slot + 1, 4>([&](auto J) { if constexpr (slot_e(V, J) != 99) halo(J); });
                vmdrain();
            }
        }
        s[n & 1] = mfma(kv[it.slot].k, q[it.unit], nm[it.unit]);
        PIN(s[n & 1]);
        if constexpr (it.merged) { sx = mfma(kv[6].k, q[it.unit], nm[it.unit]); PIN(sx); }
    };
    auto pv = [&](auto N, auto H) {
        constexpr Item it = ITEMS[V][N]; constexpr int hh = H;
        const u32x4 p = hh ? p1 : p0;
        if constexpr (it.merged) {
            const u32x4 m = {pmask<4 * hh>(), pmask<4 * hh + 1>(), pmask<4 * hh + 2>(), pmask<4 * hh + 3>()};
            const u32x4 d = p & m;
            o[it.unit] = mfma(hh ? kv[6].v1 : kv[6].v0, d, o[it.unit]);
            o[it.unit] = mfma(hh ? kv[it.slot].v1 : kv[it.slot].v0, p ^ d, o[it.unit]);
        } else o[it.unit] = mfma(hh ? kv[it.slot].v1 : kv[it.slot].v0, p, o[it.unit]);
        PIN(o[it.unit]);
    };
    qk(IC<0>{});
    SBAR();
    static_for<0, 8>([&](auto N) {
        constexpr int n = N; constexpr Item it = ITEMS[V][n];
        f32x16& c = s[n & 1];
        if constexpr (n == 2) STAMP(28 + la * 24);
        if constexpr (n == 5) STAMP(29 + la * 24);
        constexpr bool late = n + 1 == NOWN;
        if constexpr (it.merged) static_for<0, 16>([&](auto R) { constexpr int r = R; c[r] = __builtin_amdgcn_inverse_ballot_w64(diag_mask(r)) ? sx[r] : c[r]; });
        if constexpr (n + 1 < 8 && !late) qk(IC<n + 1>{});
        if constexpr (n == 1 && !G) halo(1);
        if constexpr (n >= 2) wdma(IC<(n - 2) * 2>{});
        CBAR(); SBAR();
        float e[16];
#pragma unroll
        for (int i = 0; i < 5; ++i) e[i] = opaque(__builtin_amdgcn_exp2f(c[i]));
        SBAR();
        if constexpr (n == 0) { bar(); if (HALO0 == 0) halo(0); }
        if constexpr (n > 0) pv(IC<n - 1>{}, IC<1>{});
        if constexpr (n == 1 && !G) halo(2);
        CBAR(); SBAR();
#pragma unroll
        for (int i = 5; i < 10; ++i) e[i] = __builtin_amdgcn_exp2f(c[i]);
        p0 = u32x4{pk2(e[0], e[1]), pk2(e[2], e[3]), pk2(e[4], e[5]), pk2(e[6], e[7])};
        PIN(e[8]); PIN(e[9]);
        SBAR();
        pv(N, IC<0>{});
        if constexpr (n == 1 && !G && slot_e(V, 3) != 99) halo(3);
        if constexpr (n >= 2) wdma(IC<(n - 2) * 2 + 1>{});
        CBAR(); SBAR();
#pragma unroll
        for (int i = 10; i < 16; ++i) e[i] = __builtin_amdgcn_exp2f(c[i]);
        p1 = u32x4{pk2(e[8], e[9]), pk2(e[10], e[11]), pk2(e[12], e[13]), pk2(e[14], e[15])};
        PIN(p1);
        if constexpr (n == 0 && HALO0 == 1) halo(0);
        if constexpr (n == 0 && G) { halo(1); halo(2); halo(3); }
        CBAR();
        if constexpr (late) qk(IC<n + 1>{});
        SBAR();
    });
    pv(IC<7>{}, IC<1>{});
    STAMP(31 + la * 24);
    const unsigned ex = L_EX + X.u * 6144 + X.l16, ex1 = L_EX + X.u * 6144 + 2048 + X.lane * 4;
    const f32x16& sd = o[G ^ 1];
    sts128f(ex + G * 2304, f32x4{sd[0], sd[1], sd[2], sd[3]}); sts128f(ex + G * 2304 + 1024, f32x4{sd[4], sd[5], sd[6], sd[7]});
    sts32f(ex1 + G * 2304, sd[8]);
    vmwait<8>();
    bar();
    const f32x4 r0 = lds128f(ex + (G ^ 1) * 2304), r1 = lds128f(ex + (G ^ 1) * 2304 + 1024);
    const float r2 = lds32f(ex1 + (G ^ 1) * 2304);
#pragma unroll
    for (int i = 0; i < 4; ++i) { res[i] = o[G][i] + r0[i]; res[4 + i] = o[G][4 + i] + r1[i]; }
    res[8] = o[G][8] + r2;
    qk_ = q[G];
}

template <int G> DI void outproj_exchange(const Ctx& X, const f32x16& acc, f32x4& k0, f32x4& k1) {
    const unsigned ex = L_EX + X.u * 6144 + X.l16;
    constexpr int S = 8 - 8 * G, K = 8 * G;
    sts128f(ex + (G ^ 1) * 2304, f32x4{acc[S], acc[S + 1], acc[S + 2], acc[S + 3]}); sts128f(ex + (G ^ 1) * 2304 + 1024, f32x4{acc[S + 4], acc[S + 5], acc[S + 6], acc[S + 7]});
    vmwait<0>();
    bar();
    k0 = f32x4{acc[K], acc[K + 1], acc[K + 2], acc[K + 3]} + lds128f(ex + G * 2304); k1 = f32x4{acc[K + 4], acc[K + 5], acc[K + 6], acc[K + 7]} + lds128f(ex + G * 2304 + 1024);
}
DI void outproj_finish(Ctx& X, const f32x16& acc, u32x4 xo, int li, bool last, unsigned char* out) {
    f32x4 k0, k1;
    if (X.g) outproj_exchange<1>(X, acc, k0, k1); else outproj_exchange<0>(X, acc, k0, k1);
    const u32x4 a = {pk2(k0[0], k0[1]), pk2(k0[2], k0[3]), pk2(k1[0], k1[1]), pk2(k1[2], k1[3])};
    u32x4 xr;
#pragma unroll
    for (int i = 0; i < 4; ++i) xr[i] = min(pk2(bflo(xo[i]) + bflo(a[i]), bfhi(xo[i]) + bfhi(a[i])), MARK - 1u);
    if (last) {
        *(u32x4*)(out + (size_t)(tile_of(X) * 32 + (X.lane & 31)) * 1024 + X.c * 64 + X.hi * 32 + X.g * 16) = xr;
    } else {
        stw(X, xr, X.l16, fr_so(X, OFF_X + li * MB2) + X.g * 1024);
        SBAR();
        float t1 = 0.f, t2 = 0.f;
#pragma unroll
        for (int i = 0; i < 4; ++i) {
            const unsigned n = xr[i];
            const bf16x2 v = __builtin_bit_cast(bf16x2, n);
            t1 = __builtin_amdgcn_fdot2_f32_bf16(v, __builtin_bit_cast(bf16x2, ONE2), t1, false);
            t2 = __builtin_amdgcn_fdot2_f32_bf16(v, v, t2, false);
        }
        stw32(X, __float_as_uint(xhalf_sum2(t1, t2)), (X.lane & 31) * 16 + X.hi * 4, st_so(X, li) + X.g * 8);
    }
    CBAR();
}
DI float phi1(float a, float r) {
    const float x = fmaf(a, r, 1e-6f);
    return fmaxf(x, 1e-6f) + __builtin_amdgcn_fmed3f(__builtin_amdgcn_exp2f(x * LOG2E), 0.f, 1.f);
}
DI unsigned phi2(f32x2 a, f32x2 r) {
    const f32x2 x = __builtin_elementwise_fma(a, r, f32x2{1e-6f, 1e-6f}), y = x * LOG2E;
    const f32x2 e = {__builtin_amdgcn_fmed3f(__builtin_amdgcn_exp2f(y[0]), 0.f, 1.f), __builtin_amdgcn_fmed3f(__builtin_amdgcn_exp2f(y[1]), 0.f, 1.f)};
    const f32x2 m = {fmaxf(x[0], 1e-6f), fmaxf(x[1], 1e-6f)};
    return pk2(m + e);
}

__global__ __launch_bounds__(512, 2) void fused_kernel(const unsigned short* __restrict__ xin, const unsigned short* __restrict__ wA, const unsigned short* __restrict__ woA,
                                                        const unsigned short* __restrict__ wB, const unsigned short* __restrict__ woB,
                                                        unsigned char* __restrict__ out, unsigned char* ws) {
    Ctx X;
    { const int w = __builtin_amdgcn_readfirstlane(threadIdx.x >> 6); X.u0 = w & 3; X.g0 = w >> 2; }
    STAMP(0);
    X.ws = __builtin_amdgcn_make_buffer_rsrc(ws, 0, WS_BYTES, 0x00020000);
    X.wsp = ws;
    {
        const int b = blockIdx.x, i = b >> 3; X.x0 = b & 7;
        X.t0 = (X.x0 >> 1) * 4 + (i >> 3); X.c0 = (X.x0 & 1) * 8 + (i & 7);
    }
    fresh(X);
    const int cu = X.t * 16 + X.c;
    const u32x2 gen = __builtin_amdgcn_raw_buffer_load_b64(X.ws, 0, 0, 16);

    f32x16 a0, a1, a2; float ra; f32x4 rn[4];
    auto unused_acc = [&]() {
        asm volatile("" : "=v"(a0), "=v"(a1), "=v"(a2), "=v"(ra), "=v"(rn[0]), "=v"(rn[1]), "=v"(rn[2]), "=v"(rn[3]));
    };
    auto set_epoch = [&]() {
        const unsigned long long e = (__builtin_amdgcn_readfirstlane(gen[0]) | ((unsigned long long)__builtin_amdgcn_readfirstlane(gen[1]) << 32)) + 1;
        X.e0 = (unsigned)e; X.e1 = (unsigned)(e >> 32);
    };
    auto wait_word = [&](u32x2 f, unsigned vo, unsigned so) {
#pragma nounroll
        for (unsigned spins = 0; spins < SPIN_CAP && __builtin_amdgcn_ballot_w64(f[0] != X.e0 || f[1] != X.e1) != 0; ++spins) {
            CBAR();
            f = __builtin_amdgcn_raw_buffer_load_b64(X.ws, vo, so, 16);
        }
        CBAR();
    };
    if (X.g == 0) {
        const __amdgpu_buffer_rsrc_t rx = __builtin_amdgcn_make_buffer_rsrc((void*)xin, 0, MB2, 0x00020000);
        const unsigned xs = tile_of(X) * 32768, xl = L_XI + X.u * 32768;
#pragma unroll
        for (int r = 0; r < 32; ++r)
            dma1<0, 0>(rx, X.l16 ^ ((r & 15) * 16), xs + r * 1024, xl + r * 1024);
        STAMP(1);
        vmwait<0>();
        set_epoch();
        STAMP(2);
        u32x4 xf[32];
        const int rho = X.lane & 31;
        const unsigned xb = xl + rho * 1024 + ((X.hi * 2) ^ (rho & 15)) * 16;
#pragma unroll
        for (int s = 0; s < 32; ++s) xf[s] = lds128((xb ^ ((((s >> 1) & 3) * 4 + (s & 1)) * 16)) + (s >> 3) * 256);
        bar();
        STAMP(3);
        float s1 = 0.f, s2 = 0.f;
#pragma unroll
        for (int s = 0; s < 32; ++s)
#pragma unroll
            for (int i = 0; i < 4; ++i) {
                const unsigned xw = xf[s][i];
                const bf16x2 v = __builtin_bit_cast(bf16x2, xw);
                s1 = __builtin_amdgcn_fdot2_f32_bf16(v, __builtin_bit_cast(bf16x2, ONE2), s1, false);
                s2 = __builtin_amdgcn_fdot2_f32_bf16(v, v, s2, false);
            }
        u32x4 mf;
        stats_to_frag(X, xhalf_sum(s1), xhalf_sum(s2), ra, mf);
        STAMP(4);
        wait_progress<false>(*(volatile LAS u32x4*)(size_t)L_CNT, 4);
        STAMP(8);
        gemm_qkv<0>(X, xf, 0, a0, a1, a2, ra, rn, mf);
    } else if (X.u < 3) {
        __builtin_amdgcn_s_setprio(1);
        const __amdgpu_buffer_rsrc_t rw = __builtin_amdgcn_make_buffer_rsrc((void*)wA, 0, SZ_W, 0x00020000);
        const unsigned tr = opaque(L_W + tr_lane64(X));
        {
            const int i = blockIdx.x >> 3;
#pragma unroll
            for (int r = 0; r < 8; ++r) dma1<0, 0>(rw, X.hi * 3072 + (X.lane & 31) * 16, (16 * i + 2 * r) * 3072 + X.u * 1024 + (X.c >> 3) * 512, 27648 + X.u * 1024);
            vmwait<0>();
        }
        set_epoch();
        set_progress(X, 0);
        STAMP(2);
        if (X.u == 0) {
            const unsigned wv = ((X.lane >> 2) & 7) * 3072 + X.hi * 16 * 3072 + (X.lane & 3) * 16, wsb = X.c * 64;
            auto step = [&](int s) {
                dma1<0, 0>(rw, wv, wsb + (32 * (s >> 1) + 8 * (s & 1)) * 3072, L_W + s * 3072);
                dma1<1024, 0>(rw, wv, wsb + (32 * (s >> 1) + 8 * (s & 1)) * 3072, L_W + s * 3072);
                dma1<2048, 0>(rw, wv, wsb + (32 * (s >> 1) + 8 * (s & 1)) * 3072, L_W + s * 3072);
            };
            __builtin_amdgcn_s_sleep(DELAY_W);
#pragma unroll
            for (int s = 0; s < 9; ++s) step(s);
            STAMP(1);
            bar();
            STAMP(5);
            static_for<0, 32>([&](auto S) {
                constexpr int s = S;
                if constexpr (s + 9 < 32) step(s + 9);
                vmwait<3 * ((s + 10 < 32 ? s + 10 : 32) - s - 1)>();
                set_progress(X, s + 1);
            });
            set_progress(X, 33);
            STAMP(7);
#pragma unroll
            for (int i = 0; i < 6; ++i) stw(X, tr_frag(tr + X.t * 6144 + i * 1024, 256), X.l16 + (i & 3) * 1024, OFF_W + X.c * 98304 + X.t * 6144 + (i >> 2) * 4096);
        } else {
            bar();
            float c0 = 0.f, c1 = 0.f, d0 = 0.f, d1 = 0.f;
            const unsigned ta = opaque(tr + (X.u - 1) * 1024), tb = opaque(tr + 2048 + (X.u - 1) * 3072);
#pragma nounroll
            for (int s = 0; s < 32; s += 4) {
                wait_progress<false>(*(volatile LAS u32x4*)(size_t)L_CNT, s + 4);
                const u32x4 f0 = tr_frag(ta + s * 3072, 256), f1 = tr_frag(ta + s * 3072 + 3072, 256), f2 = tr_frag(ta + s * 3072 + 6144, 256), f3 = tr_frag(ta + s * 3072 + 9216, 256);
                const u32x4 f4 = tr_frag(tb + s * 3072, 256), f5 = tr_frag(tb + s * 3072 + 6144, 256);
                colsum_add(f0, c0, c1); colsum_add(f1, c0, c1); colsum_add(f2, c0, c1); colsum_add(f3, c0, c1);
                colsum_add(f4, d0, d1); colsum_add(f5, d0, d1);
            }
            colsum_frag(X, 0, X.u - 1, xhalf_sum(c0 + c1));
            const float d = xhalf_sum(d0 + d1);
            if (X.u == 2) { sts32f(L_MH + X.lane * 4, d); lgkwait<0>(); set_progress(X, 33); }
            else {
#pragma nounroll
                for (unsigned spins = 0; spins < SPIN_CAP * 16 && __builtin_amdgcn_readfirstlane(*(volatile LAS unsigned*)(size_t)(L_CNT + 8)) < 33; ++spins) __builtin_amdgcn_s_sleep(1);
                CBAR();
                colsum_frag(X, 0, 2, d + lds32f(L_MH + X.lane * 4));
                lgkwait<0>();
                set_progress(X, 33);
            }
        }
        wait_word(__builtin_amdgcn_raw_buffer_load_b64(X.ws, X.lane * 8, OFF_START + X.u * 512, 16), X.lane * 8, OFF_START + X.u * 512);
        unused_acc();
    } else {
        const int ps = cu >> 3, pg = cu & 7;
        const __amdgpu_buffer_rsrc_t rb = __builtin_amdgcn_make_buffer_rsrc((void*)wB, 0, SZ_W, 0x00020000);
        const __amdgpu_buffer_rsrc_t roa = __builtin_amdgcn_make_buffer_rsrc((void*)woA, 0, SZ_WO, 0x00020000);
        const __amdgpu_buffer_rsrc_t rob = __builtin_amdgcn_make_buffer_rsrc((void*)woB, 0, SZ_WO, 0x00020000);
        auto marks0 = [&]() {
#pragma unroll
            for (int u = 0; u < 4; ++u) { X.u = u; mark2(X, kv_so(X, 0)); mark2(X, kv_so(X, 0) + 2048); }
        };
        auto marks1 = [&]() {
#pragma unroll
            for (int u = 0; u < 4; ++u) { X.u = u; mark2(X, fr_so(X, OFF_O)); }
#pragma unroll
            for (int lb = 0; lb < 2; ++lb) {
                mark2(X, kvp_so(X, lb));
                stw(X, splat4(MARK), X.lane < 8 ? X.l16 + 2048 : OOB, kvp_so(X, lb));
                stw(X, splat4(MARK), kvf_vo(X, 0), OFF_KVF + lb * 8 * SZ_KVF);
                stw(X, splat4(MARK), kvf_vo(X, 1), X.t == X.c ? OFF_KVF + lb * 8 * SZ_KVF : OOB);
            }
            stw(X, splat4(ONE2), X.lane < 2 ? X.lane * 512 : OOB, OFF_ONES + cu * 1024);
        };
        auto marks2 = [&]() {
#pragma unroll
            for (int u = 0; u < 4; ++u) { X.u = u; mark_x(X, 0); }
        };
        STAMPU(4, 3);
        if (MARK_EARLY >= 1) marks0();
        if (MARK_EARLY >= 2) marks1();
        if (MARK_EARLY >= 3) marks2();
        bar();
#pragma unroll
        for (int p = 0; p < 2; ++p) {
            dma1<0, 0>(roa, (X.lane >> 3) * 1024 + (X.lane & 7) * 16, (16 * ps + 8 * p) * 1024 + pg * 128, L_RT + 6144 + p * 1024);
            dma1<0, 0>(rob, (X.lane >> 3) * 1024 + (X.lane & 7) * 16, (16 * ps + 8 * p) * 1024 + pg * 128, L_RT + 8192 + p * 1024);
#pragma unroll
            for (int part = 0; part < 3; ++part)
                dma1<0, 0>(rb, (X.lane >> 3) * 3072 + (X.lane & 7) * 16, (32 * (ps >> 1) + 8 * (ps & 1) + 16 * p) * 3072 + part * 1024 + pg * 128, L_RT + part * 2048 + p * 1024);
        }
        if (MARK_EARLY < 1) marks0();
        vmwait<(MARK_EARLY < 1 ? 16 : 0)>();
        set_epoch();
        if (X.lane < 2) sts128(L_ONES + X.l16, splat4(ONE2));
        {
            const int b = (X.lane >> 4) & 1, q = (X.lane >> 2) & 3, p = X.lane & 3;
            const unsigned trw = L_RT + (8 * X.hi + q) * 128 + b * 32 + p * 8, tro = L_RT + 6144 + (4 * X.hi + q) * 128 + (16 * (p & 1) + 8 * b + 4 * (p >> 1)) * 2;
#pragma unroll
            for (int cc = 0; cc < 2; ++cc) {
                stw(X, tr_frag(tro + cc * 64, 1024), X.l16, OFF_WO + ((2 * pg + cc) * 32 + ps) * 1024);
                stw(X, tr_frag(tro + 2048 + cc * 64, 1024), X.l16, OFF_WO + SZ_WO + ((2 * pg + cc) * 32 + ps) * 1024);
#pragma unroll
                for (int part = 0; part < 3; ++part) stw(X, tr_frag(trw + part * 2048 + cc * 64, 512), X.l16 + part * 1024, OFF_W + SZ_W + ((2 * pg + cc) * 96 + ps * 3) * 1024);
            }
        }
        if (MARK_EARLY < 2) marks1();
        if (MARK_EARLY < 3) marks2();
        vmwait<0>();
        flag_store(X, OFF_START + cu * 8);
        STAMPU(6, 3);
        wait_word(__builtin_amdgcn_raw_buffer_load_b64(X.ws, X.lane * 8, OFF_START + 3 * 512, 16), X.lane * 8, OFF_START + 3 * 512);
        unused_acc();
    }

    for (int it = 0;; ++it) {
        const int sb = 9 + it * 24; (void)sb;
        STAMP(sb + 0);
        {
            fresh(X);
            if (X.g == 0) {
                const unsigned so = kv_so(X, it), lb = L_QKV + X.u * 6144 + X.l16;
                const unsigned hb = (X.lane >> 4) & 1, ch = X.hi * 16 + (X.lane & 15);
                const unsigned lv = L_QKV + X.u * 6144 + hb * 3072 + ch * 32;
                const u32x4 k0 = pack8s(a1, 0, ra);
                stw(X, k0, X.l16, so); sts128(lb + 1024, k0); SBAR();
                const u32x4 k1 = pack8s(a1, 8, ra);
                stw(X, k1, X.l16 + 2048, so); sts128(lb + 4096, k1); SBAR();
                const u32x4 v0 = pack8r(a2, 0, rn);
                stw(X, v0, hb * 2048 + ch * 16 + 1024, so); sts128(lv + 2048, v0); SBAR();
                const u32x4 v1 = pack8r(a2, 8, rn);
                stw(X, v1, hb * 2048 + ch * 16 + 1536, so); sts128(lv + 2064, v1); SBAR();
                const float rq = ra * QSCALE;
                sts128(lb, pack8s(a0, 0, rq)); SBAR();
                sts128(lb + 3072, pack8s(a0, 8, rq)); SBAR();
                f32x2 m0 = pair(a0, 0) * pair(a1, 0), m1 = pair(a0, 8) * pair(a1, 8);
#pragma unroll
                for (int j = 2; j < 8; j += 2) { m0 += pair(a0, j) * pair(a1, j); m1 += pair(a0, 8 + j) * pair(a1, 8 + j); }
                sts32f(L_MH + X.u * 256 + X.lane * 4, __uint_as_float(pk2(xhalf_sum2(m0[0] + m0[1], m1[0] + m1[1]) * -(ra * rq), 0.f)));
            }
            STAMP(sb + 1);
            const KVaddr ka = kv_addr(X, it);
            {
                u32x4 q; f32x16 k;
                const int qt = X.g ? 3 - (X.u >> 1) : (X.u >> 1);
                const unsigned oso = OFF_O + 2 * it * MB2 + (4 * X.t + qt) * 32768 + (2 * X.c + (X.u & 1)) * 1024;
                if (X.u < 2) { if (X.g == 0) attention<0>(X, it, ka, q, k); else attention<1>(X, it, ka, q, k); }
                else { if (X.g == 0) attention<2>(X, it, ka, q, k); else attention<3>(X, it, ka, q, k); }
                const float sa = (fabsf(k[0]) + fabsf(k[1])) + (fabsf(k[2]) + fabsf(k[3])) + (fabsf(k[4]) + fabsf(k[5])) + (fabsf(k[6]) + fabsf(k[7]));
                if (UNLIKELY(__builtin_amdgcn_ballot_w64(!(sa < 0x1p120f) || !(k[8] < 0x1p100f) || !(k[8] > 0x1p-20f)) != 0)) { COUNT(5); att_slow(X, ka, qt, q, k); }
                stw(X, pack8s(k, 0, __builtin_amdgcn_rcpf(k[8])), X.l16, oso);
            }
            CBAR();
            __builtin_amdgcn_s_setprio(0);
            STAMP(sb + 3);
            fresh(X);
            const unsigned ob = OFF_O + 2 * it * MB2 + tile_of(X) * 32768 + X.g * 16384;
            u32x4 xo;
            if (it == 0) {
                xo = *(const u32x4*)((const unsigned char*)xin + (size_t)(tile_of(X) * 32 + (X.lane & 31)) * 1024 + X.c * 64 + X.hi * 32 + X.g * 16);
                dma_lin<8, 12>(X, OFF_W + SZ_W + X.c * 98304 + (X.g * 4 + X.u) * 12288, L_W + (X.g * 4 + X.u) * 12288);
                __builtin_amdgcn_s_sleep(DELAY_O > 6 ? DELAY_O - 6 : 0);
            } else {
                xo = ldw(X, X.l16, fr_so(X, OFF_X + MB2) + X.g * 1024);
                __builtin_amdgcn_s_sleep(DELAY_O);
            }
            u32x4 w[3], of[16];
            const unsigned wo = L_WO + X.g * 16384 + X.l16;
            w[0] = lds128(wo); w[1] = lds128(wo + 1024);
            CBAR();
            ld_range<0, OD>(X, of, ob);
            CBAR();
            f32x16 acc = zero16();
            static_for<0, 16>([&](auto S) {
                constexpr int s = S;
                if constexpr (!(s & 1)) verify_pair<s / 2, (s + OD < 16 ? s + OD : 16), 0>(X, of, ob, umax8(of[s], of[s + 1]));
                acc = mfma(w[s % 3], of[s], acc);
                PIN(acc);
                if constexpr (s + 2 < 16) w[(s + 2) % 3] = lds128(wo + (s + 2) * 1024);
                if constexpr (s + OD < 16) ld_range<s + OD, s + OD + 1>(X, of, ob);
                CBAR(); SBAR();
            });
            STAMP(sb + 4);
            outproj_finish(X, acc, xo, 2 * it, false, out);
            STAMP(sb + 5);
        }
        {
            fresh(X);
            const unsigned pb = OFF_O + (2 * it + 1) * MB2 + tile_of(X) * 32768 + X.g * 16384;
            if (X.g == 0) {
                u32x4 xf[32];
                __builtin_amdgcn_s_sleep(DELAY_X);
                CBAR();
                STAMP(sb + 6);
                gemm_qkv<2>(X, xf, 2 * it, a0, a1, a2, ra, rn, splat4(0));
                STAMP(sb + 7);
                u32x4 k0, k1;
#pragma unroll
                for (int i = 0; i < 4; ++i) k0[i] = phi2(pair(a1, 2 * i), rn2(rn, 2 * i));
#pragma unroll
                for (int i = 0; i < 4; ++i) k1[i] = phi2(pair(a1, 8 + 2 * i), rn2(rn, 8 + 2 * i));
                const u32x4 v0 = pack8r(a2, 0, rn), v1 = pack8r(a2, 8, rn);
                SBAR();
                f32x16 kv = mfma(k0, v0, zero16());
                kv = mfma(k1, v1, kv);
                f32x16 zz = mfma(k0, splat4(ONE2), zero16());
                zz = mfma(k1, splat4(ONE2), zz);
                PIN(kv); PIN(zz);
                const bool hbit = X.lane & 16;
                const unsigned rb = L_RED + X.u * 6144 + X.lane * 32, rz = (X.lane & 15) == 0 ? L_RED + X.u * 6144 + 2048 + X.lane * 2 : L_RED + X.u * 6144 + 4096 + X.lane * 32;
                f32x4 e0, e1, z0, z1;
#pragma unroll
                for (int j = 0; j < 4; ++j) { const float p0 = opaque(kv[8 + j]), p1 = opaque(kv[12 + j]); e0[j] = hbit ? p0 : kv[j]; e1[j] = hbit ? p1 : kv[4 + j]; }
                sts128f(rb, e0); sts128f(rb + 16, e1);
#pragma unroll
                for (int j = 0; j < 4; ++j) { const float p2 = opaque(zz[8 + j]), p3 = opaque(zz[12 + j]); z0[j] = hbit ? p2 : zz[j]; z1[j] = hbit ? p3 : zz[4 + j]; }
                sts128f(rz, z0); sts128f(rz + 16, z1);
                STAMP(sb + 8);
                bar();
                const unsigned po = fr_so(X, OFF_O + (2 * it + 1) * MB2);
                u32x4 p0, p1;
#pragma unroll
                for (int i = 0; i < 4; ++i) { p0[i] = phi2(pair(a0, 2 * i), f32x2{ra, ra}); p1[i] = phi2(pair(a0, 8 + 2 * i), f32x2{ra, ra}); }
                PIN(p0); PIN(p1);
                __builtin_amdgcn_s_sleep(DELAY_PQ);
                stw(X, p0, X.l16, po); stw(X, p1, X.l16 + 1024, po);
                STAMP(sb + 10);
                u32x4 g[5];
                const unsigned gs = sopaque(OFF_KVF + (it * 8 + X.x0) * SZ_KVF + X.u * 4096), gz = sopaque(X.u == 0 ? gs + 16384 : OOB);
                vmwait<0>();
                bar();
                STAMP(sb + 12);
                __builtin_amdgcn_s_sleep(DELAY_KVF);
                CBAR();
#pragma unroll
                for (int i = 0; i < 4; ++i) g[i] = ldw(X, X.l16 + i * 1024, gs);
                g[4] = ldw(X, X.l16, gz);
                CBAR();
                if (UNLIKELY(marked(umax3(umax8(g[0], g[1]), umax8(g[2], g[3]), umax4(g[4]))))) {
#pragma nounroll
                    for (unsigned spins = 0; spins < SPIN_CAP; ++spins) {
                        COUNT(7);
                        __builtin_amdgcn_s_sleep(2);
                        CBAR();
#pragma unroll
                        for (int i = 0; i < 4; ++i) if (umax4(g[i]) == MARK) g[i] = ldw(X, X.l16 + i * 1024, gs);
                        if (umax4(g[4]) == MARK) g[4] = ldw(X, X.l16, gz);
                        CBAR();
                        if (!marked(umax3(umax8(g[0], g[1]), umax8(g[2], g[3]), umax4(g[4])))) break;
                    }
                    vmdrain();
                }
#pragma unroll
                for (int i = 0; i < 4; ++i) sts128(L_KVF + X.u * 4096 + X.l16 + i * 1024, g[i]);
                if (X.u == 0) sts128(L_KVF + 16384 + X.l16, g[4]);
            } else {
                if (it == 0 && X.u < 3) {
                    if (X.u == 0) { vmwait<0>(); flag_store(X, OFF_WFLAG + (X.c * 16 + X.t) * 8); }
                    float c0 = 0.f, c1 = 0.f;
                    const unsigned a = L_W + X.u * 1024 + X.l16, a2_ = opaque(a + 49152);
#pragma unroll
                    for (int s = 0; s < 32; ++s) colsum_add(lds128(s < 16 ? a + s * 3072 : a2_ + (s - 16) * 3072), c0, c1);
                    colsum_frag(X, 1, X.u, xhalf_sum(c0 + c1));
                    lgkwait<0>();
                    set_progress(X, 34);
                } else __builtin_amdgcn_s_sleep(DELAY_WO);
                dma_lin<0, 8>(X, OFF_WO + SZ_WO + X.c * 32768 + X.u * 8192, L_WO + X.u * 8192);
                if (it == 0) {
                    mark_x(X, 1); mark2(X, kv_so(X, 1)); mark2(X, kv_so(X, 1) + 2048);
                    const unsigned fv = (X.lane & 15) * 8;
                    wait_word(__builtin_amdgcn_raw_buffer_load_b64(X.ws, fv, OFF_WFLAG + X.c * 128, 16), fv, OFF_WFLAG + X.c * 128);
                }
                vmwait<0>();
                STAMP(sb + 8);
                if (X.u < 2) {
                    const unsigned o = opaque(L_RED + X.u * 1024 + X.l16), so = sopaque(kvp_so(X, it)), zo = opaque(X.u == 0 && X.lane < 8 ? X.l16 + 2048 : OOB);
                    const unsigned wg = sopaque(OFF_W + X.c * 98304 + X.u * 49152), wl = sopaque(L_W + X.u * 49152);
                    bar();
                    const f32x4 s = (lds128f(o) + lds128f(o + 6144)) + (lds128f(o + 2 * 6144) + lds128f(o + 3 * 6144));
                    stw(X, __builtin_bit_cast(u32x4, s), X.l16 + X.u * 1024, so);
                    SBAR();
                    const f32x4 z = (lds128f(o + 2048) + lds128f(o + 2048 + 6144)) + (lds128f(o + 2048 + 2 * 6144) + lds128f(o + 2048 + 3 * 6144));
                    stw(X, __builtin_bit_cast(u32x4, z), zo, so);
                    CBAR();
                    STAMP(sb + 9);
                    if (it == 0)
                        static_for<0, WA_EARLY>([&](auto I) { dma_lin<I, I + 1>(X, wg, wl); vmwait<WA_DEPTH>(); });
                    bar();
                } else if (X.u == 3 || X.t == X.c) {
                    const unsigned fo = opaque((X.u == 2 ? 2048u : (unsigned)X.t * 128u) + (X.lane & 15) * 2304 + (X.lane >> 4) * 32), fs = sopaque(OFF_KVP + it * SZ_KVP + X.c * 16 * 2304);
                    const unsigned ko = opaque(kvf_vo(X, X.u == 2));
                    bar();
                    __builtin_amdgcn_s_sleep(DELAY_KVP);
                    CBAR();
                    STAMPU(sb + 6, 3);
                    u32x4 p0 = ldw(X, fo, fs), p1 = ldw(X, fo + 16, fs);
#pragma nounroll
                    for (unsigned spins = 0; spins < SPIN_CAP; ++spins) {
                        const bool miss = umax8(p0, p1) == MARK;
                        if (__builtin_amdgcn_ballot_w64(miss) == 0) break;
                        COUNT(6);
                        __builtin_amdgcn_s_sleep(KVP_BACKOFF);
                        CBAR();
                        if (miss) { p0 = ldw(X, fo, fs); p1 = ldw(X, fo + 16, fs); }
                        CBAR();
                    }
                    STAMPU(sb + 10, 3);
                    float v[8];
#pragma unroll
                    for (int i = 0; i < 4; ++i) { v[i] = __uint_as_float(p0[i]); v[4 + i] = __uint_as_float(p1[i]); }
                    static_for<0, 4>([&](auto K) {
#pragma unroll
                        for (int i = 0; i < 8; ++i) v[i] += __builtin_amdgcn_mov_dpp(v[i], 0x120 + (8 >> K), 0xF, 0xF, true);
                    });
                    u32x4 r = {pk2(v[0], v[1]), pk2(v[2], v[3]), pk2(v[4], v[5]), pk2(v[6], v[7])};
                    PIN(r);
                    bar();
                    stw(X, r, ko, OFF_KVF + it * 8 * SZ_KVF);
                    CBAR();
                } else { bar(); bar(); }
            }
            STAMP(sb + 13);
            bar();
            STAMP(sb + 14);
            u32x4 pf[16];
            ld_range<0, PD>(X, pf, pb);
            const u32x4 xo = ldw(X, X.l16, fr_so(X, OFF_X + 2 * it * MB2) + X.g * 1024);
            CBAR();
            constexpr int WA_LATE = (48 - WA_EARLY) / 4;
            const unsigned wpart = X.g * 49152 + (WA_EARLY + X.u * WA_LATE) * 1024;
            const unsigned wg = it == 0 ? OFF_W + X.c * 98304 + wpart : OOB, wl = it == 0 ? L_W + wpart : LDS_BYTES;
            const unsigned mo = it == 0 ? fr_so(X, OFF_O + 2 * MB2) + X.g * 1024 : OOB;
            const unsigned ko = opaque(L_KVF + X.g * 16 * 544 + ((X.lane & 16) ? 512 + X.hi * 16 : (X.hi * 16 + (X.lane & 15)) * 16)), wo = L_WO + X.g * 16384 + X.l16;
            f32x16 acc = zero16();
            {
                u32x4 kf[4], wf[3], of;
                f32x16 nd[3];
                float inv;
                kf[0] = lds128(ko); kf[1] = lds128(ko + 544); kf[2] = lds128(ko + 2 * 544); kf[3] = lds128(ko + 3 * 544); wf[0] = lds128(wo); wf[1] = lds128(wo + 1024);
                nd[0] = mfma(kf[0], pf[0], zero16());
                nd[1] = mfma(kf[1], pf[1], zero16());
                PIN(nd[0]); PIN(nd[1]);
                inv = __builtin_amdgcn_rcpf(nd[0][8] + 1e-6f);
                SBAR();
                static_for<0, 17>([&](auto H) {
                    constexpr int h = H;
                    float t[8];
                    if constexpr (h + 2 < 16) { nd[(h + 2) % 3] = mfma(kf[(h + 2) & 3], pf[(h + 2) & 15], zero16()); PIN(nd[(h + 2) % 3]); }
                    if constexpr (h + 4 < 16) kf[h & 3] = lds128(ko + (h + 4) * 544);
                    if constexpr (h + PD < 16) ld_range<h + PD, h + PD + 1>(X, pf, pb);
                    if constexpr (h < 16) {
#pragma unroll
                        for (int i = 0; i < 8; ++i) t[i] = opaque(nd[h % 3][i] * inv);
                        asm volatile("" :: "v"(nd[h % 3]));
                    }
                    CBAR(); SBAR();
                    if constexpr (h > 0) { acc = mfma(wf[(h - 1) % 3], of, acc); PIN(acc); }
                    if constexpr (h > 0 && h + 1 < 16) wf[(h + 1) % 3] = lds128(wo + (h + 1) * 1024);
                    if constexpr (h >= 16 - WA_LATE && h < 16) dma1<0, 16>(X.ws, X.l16, wg + (15 - h) * 1024, wl + (15 - h) * 1024);
                    if constexpr (h == 12) stw(X, splat4(MARK), X.l16, mo);
                    if constexpr (h + 1 < 16) inv = opaque(__builtin_amdgcn_rcpf(nd[(h + 1) % 3][8] + 1e-6f));
                    if constexpr (h < 16) { of = u32x4{pk2(t[0], t[1]), pk2(t[2], t[3]), pk2(t[4], t[5]), pk2(t[6], t[7])}; PIN(of); }
                    CBAR(); SBAR();
                });
            }
            STAMP(sb + 15);
            bar();
            outproj_finish(X, acc, xo, 1, it == 1, out);
            STAMP(sb + 16);
            if (it == 1) break;
            fresh(X);
            if (X.g == 0) {
                u32x4 xf[32];
                __builtin_amdgcn_s_sleep(DELAY_X);
                CBAR();
                STAMP(sb + 17);
                gemm_qkv<1>(X, xf, 1, a0, a1, a2, ra, rn, splat4(0));
            } else {
                __builtin_amdgcn_s_sleep(DELAY_WO);
                dma_lin<0, 8>(X, OFF_WO + X.c * 32768 + X.u * 8192, L_WO + X.u * 8192);
                mark_x(X, 2);
                vmwait<0>();
                unused_acc();
            }
        }
    }
    fresh(X);
    if (X.t == 0 && X.c == 0 && X.u == 0 && X.g == 0) flag_store(X, 0);
}

extern "C" void kernel_launch(void* const* d_in, const int* in_sizes, int n_in, void* d_out, int out_size, void* d_ws, size_t ws_size, hipStream_t stream) {
    (void)in_sizes; (void)n_in; (void)out_size; (void)ws_size;
    (void)hipFuncSetAttribute((const void*)fused_kernel, hipFuncAttributeMaxDynamicSharedMemorySize, LDS_BYTES);
    fused_kernel<<<256, 512, LDS_BYTES, stream>>>((const unsigned short*)d_in[0], (const unsigned short*)d_in[1], (const unsigned short*)d_in[2],
                                                  (const unsigned short*)d_in[4], (const unsigned short*)d_in[5], (unsigned char*)d_out, (unsigned char*)d_ws + WS_SHIFT);
}
```

```cpp
#define OD 12
#define OR 1
#include <hip/hip_runtime.h>
#include <hip/hip_bf16.h>
#include <type_traits>

typedef __bf16 bf16x8 __attribute__((ext_vector_type(8)));
typedef __bf16 bf16x2 __attribute__((ext_vector_type(2)));
typedef float f32x16 __attribute__((ext_vector_type(16)));
typedef float f32x4 __attribute__((ext_vector_type(4)));
typedef float f32x2 __attribute__((ext_vector_type(2)));
typedef unsigned u32x4 __attribute__((ext_vector_type(4)));
typedef unsigned u32x2 __attribute__((ext_vector_type(2)));
typedef short s16x4 __attribute__((ext_vector_type(4)));
#define GAS __attribute__((address_space(1)))
#define LAS __attribute__((address_space(3)))
typedef GAS unsigned long long gu64;
#define DI __device__ __forceinline__
#define RLX_AGENT __ATOMIC_RELAXED, __HIP_MEMORY_SCOPE_AGENT
#define CBAR() asm volatile("" ::: "memory")
#define SBAR() __builtin_amdgcn_sched_barrier(0)
#define PIN(x) asm volatile("" : "+v"(x))
#define UNLIKELY(x) __builtin_expect(!!(x), 0)
template <int N> using IC = std::integral_constant<int, N>;

constexpr unsigned OFF_WFLAG = 65536;
constexpr unsigned OFF_HOP = 320u << 10;
constexpr unsigned OFF_AH = 544u << 10;
constexpr unsigned OFF_KVFLAG = 576u << 10;
constexpr unsigned OFF_ONES = 896u << 10;
constexpr unsigned OFF_W = 2u << 20;
constexpr unsigned SZ_W = 1572864;
constexpr unsigned OFF_WO = OFF_W + 2 * SZ_W;
constexpr unsigned SZ_WO = 524288;
constexpr unsigned OFF_CSP = 12u << 20;
constexpr unsigned SZ_CSP = 16 * 32 * 384;
constexpr unsigned OFF_X = 6u << 20;
constexpr unsigned OFF_KV = 13u << 20;
constexpr unsigned OFF_O = 21u << 20;
constexpr unsigned OFF_KVP = 29u << 20;
constexpr unsigned SZ_KVP = 16 * 16 * 2304;
constexpr unsigned OFF_ST = 32u << 20;
constexpr unsigned SZ_ST = 524288;
constexpr unsigned WS_BYTES = 34u << 20;
constexpr unsigned MB2 = 2u << 20;
#define REUSE 0
constexpr unsigned STEP_XO = REUSE ? 0 : MB2, STEP_KV = REUSE ? 0 : 2 * MB2, STEP_ST = REUSE ? 0 : SZ_ST;
constexpr unsigned OOB = 0x80000000u;
constexpr unsigned MARK = 0xffffffffu;

constexpr unsigned L_W = 0, L_WO = 98304, L_S = 131072;
constexpr unsigned L_XI = 31744;
constexpr unsigned L_QKV = L_S, L_MH = L_S + 24576, L_STAT = L_S + 25600, L_CSF = L_S + 26112;
constexpr unsigned L_MEAN = L_MH;
constexpr unsigned L_RED = L_S, L_KVF = L_MH - 64, L_EX = L_S;
constexpr unsigned L_ONES = L_CSF + 6144;
constexpr unsigned L_CNT = L_ONES + 32;
constexpr unsigned L_XR = L_CNT + 16;
constexpr unsigned L_SF = L_XR + 4;
constexpr unsigned L_DBG = L_ONES + 56;
constexpr unsigned LDS_BYTES = L_ONES + 512;

constexpr float QSCALE = 0.25f * 1.4426950408889634f;
constexpr float LOG2E = 1.4426950408889634f;
constexpr unsigned SPIN_CAP = 1u << 18;
constexpr unsigned ONE2 = 0x3f803f80u;

#define DELAY_O 12
#define DELAY_X 13
#define DELAY_KVP 8
#define DELAY_KVF 8
#define DELAY_W 4
#define DELAY_PQ 3
#define DELAY_WO 24
#define DELAY_MK 24
#define MAPPING 1
#define WA_EARLY 16
#define WA_DEPTH 4
#define ATT_PRIO 1
#define MARK_EARLY 0
#define KVP_BACKOFF 3
#define PD 8
#define TOUCH 0
#define WS_SHIFT 0
#define DELAY_H 3
#define DELAY_G 0
#define BACKOFF 1
#define DELAY_KV 0
#define KVFLAG_AT 16
#define DELAY_WQ 16
#define NPOLL 1
#define PGAP 3
#define WA_STEPS 6
#define DELAY_W2 6
#define DELAY_R 0
#define ATT_SWITCH 5
#define ROT 0
#define DELAY_ST 16
#define H3_AT 30
#define OHINT_ATOMIC 1
#define DELAY_KVQ 0
#define DELAY_SIDE 5
#define CODEWARM 46
#define HOPS 1
#define OB_TIMED 20
#define DELAY_Q3 24
#define ROTO 0
#define XD 8
#define XR 4
#define HALO0 -1
#define PQ_SPLIT 8
#define STAMP(i) do {} while (0)
#define STAMPU(i, u) do {} while (0)
#define COUNT(i) do {} while (0)

template <int N> __device__ __forceinline__ void pad() { asm volatile(".Lhop%c0:\n\ts_cbranch_execz .Lhop%c1" :: "i"(N), "i"(N + 1)); }
template <int A, int B> __device__ __forceinline__ void hop_through() {
    asm volatile("s_mov_b64 s[100:101], exec\n\ts_mov_b64 exec, 0\n\ts_branch .Lhop%c0\n.Lhop%c1:\n\ts_mov_b64 exec, s[100:101]" :: "i"(A), "i"(B) : "s100", "s101", "memory");
}

struct Ctx {
    __amdgpu_buffer_rsrc_t ws;
    unsigned char* wsp;
    unsigned e0, e1;
    int t, c, u, g, lane, hi;
    unsigned l16;
    int t0, c0, u0, g0, x0;
};

DI f32x16 mfma(u32x4 a, u32x4 b, f32x16 c) {
    return __builtin_amdgcn_mfma_f32_32x32x16_bf16(__builtin_bit_cast(bf16x8, a), __builtin_bit_cast(bf16x8, b), c, 0, 0, 0);
}
DI unsigned pk2(float a, float b) { bf16x2 v = {(__bf16)a, (__bf16)b}; return __builtin_bit_cast(unsigned, v); }
DI unsigned pk2(f32x2 a) { return pk2(a[0], a[1]); }
DI float bflo(unsigned x) { return __uint_as_float(x << 16); }
DI float bfhi(unsigned x) { return __uint_as_float(x & 0xffff0000u); }
DI float xhalf_sum2(float a, float b) {
    auto r = __builtin_amdgcn_permlane32_swap(__float_as_uint(a), __float_as_uint(b), false, false); return __uint_as_float(r[0]) + __uint_as_float(r[1]);
}
DI float xhalf_sum(float x) { return xhalf_sum2(x, x); }
DI float xhalf_max(float x) { auto r = __builtin_amdgcn_permlane32_swap(__float_as_uint(x), __float_as_uint(x), false, false); return fmaxf(__uint_as_float(r[0]), __uint_as_float(r[1])); }
DI u32x4 ldw(const Ctx& X, unsigned voff, unsigned soff) { return __builtin_amdgcn_raw_buffer_load_b128(X.ws, voff, soff, 16); }
DI void stw(const Ctx& X, u32x4 v, unsigned voff, unsigned soff) {
    __builtin_amdgcn_raw_buffer_store_b128(v, X.ws, voff, soff, 16);
    asm volatile("s_nop 1" :: "v"(v));
}
DI void stw32(const Ctx& X, unsigned v, unsigned voff, unsigned soff) { __builtin_amdgcn_raw_buffer_store_b32(v, X.ws, voff, soff, 16); }
DI u32x4 lds128(unsigned a) { return *(const LAS u32x4*)(size_t)a; }
DI f32x4 lds128f(unsigned a) { return *(const LAS f32x4*)(size_t)a; }
DI float lds32f(unsigned a) { return *(const LAS float*)(size_t)a; }
DI void sts128(unsigned a, u32x4 v) { *(LAS u32x4*)(size_t)a = v; }
DI void sts128f(unsigned a, f32x4 v) { *(LAS f32x4*)(size_t)a = v; }
DI void sts32f(unsigned a, float v) { *(LAS float*)(size_t)a = v; }
DI void bar() { asm volatile("s_waitcnt lgkmcnt(0)\n\ts_barrier" ::: "memory"); }
template <int N> DI void vmwait() { asm volatile("s_waitcnt vmcnt(%0)" :: "n"(N) : "memory"); }
DI void vmdrain() { CBAR(); __builtin_amdgcn_s_waitcnt(0x0F70); CBAR(); }
template <int N> DI void lgkwait() { __builtin_amdgcn_s_waitcnt(0xC07F | (N << 8)); }
DI f32x16 zero16() { f32x16 z; _Pragma("unroll") for (int i = 0; i < 16; ++i) z[i] = 0.f; return z; }
DI u32x4 splat4(unsigned v) { u32x4 r = {v, v, v, v}; return r; }
DI unsigned umax3(unsigned a, unsigned b, unsigned c) { return max(max(a, b), c); }
DI unsigned umax4(u32x4 a) { return max(umax3(a[0], a[1], a[2]), a[3]); }
DI unsigned umax8(u32x4 a, u32x4 b) { return max(umax3(umax3(umax3(a[0], a[1], a[2]), a[3], b[0]), b[1], b[2]), b[3]); }
DI bool marked(unsigned m) { return __builtin_amdgcn_ballot_w64(m == MARK) != 0; }
DI float opaque(float x) { PIN(x); return x; }
DI unsigned opaque(unsigned x) { PIN(x); return x; }
DI unsigned sopaque(unsigned x) { asm volatile("" : "+s"(x)); return x; }
DI f32x2 pair(const f32x16& a, int i) { f32x2 r = {a[i], a[i + 1]}; return r; }
template <int I, int N, class F> DI void static_for(F&& f) { if constexpr (I < N) { f(IC<I>{}); static_for<I + 1, N>(f); } }

DI void fresh(Ctx& X) {
    X.lane = __builtin_amdgcn_mbcnt_hi(~0u, __builtin_amdgcn_mbcnt_lo(~0u, 0u));
    PIN(X.lane);
    X.t = X.t0; X.c = X.c0; X.u = X.u0; X.g = X.g0;
    asm volatile("" : "+s"(X.t), "+s"(X.c), "+s"(X.u), "+s"(X.g));
    X.hi = X.lane >> 5; X.l16 = X.lane * 16;
}
template <int IMM, int AUX> DI void dma1(__amdgpu_buffer_rsrc_t r, unsigned voff, unsigned soff, unsigned lds) {
    __builtin_amdgcn_raw_ptr_buffer_load_lds(r, (LAS void*)(size_t)lds, 16, voff, soff, IMM, AUX);
}
template <int A, int B> DI void dma_lin(const Ctx& X, unsigned goff, unsigned loff) {
    static_for<A, B>([&](auto I) { constexpr int i = I; dma1<(i & 3) * 1024, 16>(X.ws, X.l16, goff + (i >> 2) * 4096, loff + (i >> 2) * 4096); });
}
DI void flag_store(const Ctx& X, unsigned off) {
    if (X.lane == 0) __hip_atomic_store((gu64*)(X.wsp + off), (unsigned long long)X.e0 | ((unsigned long long)X.e1 << 32), RLX_AGENT);
    CBAR();
}

template <int N = 1, int GAP = 3>
DI void wait_word(const Ctx& X, unsigned vo, unsigned so, int cnt) {
    const bool on = vo < OOB;
#pragma nounroll
    for (unsigned spins = 0; spins < SPIN_CAP; ++spins) {
        u32x2 f[N];
#pragma unroll
        for (int i = 0; i < N; ++i) {
            CBAR();
            f[i] = __builtin_amdgcn_raw_buffer_load_b64(X.ws, vo, so, 16);
            CBAR();
            if (i + 1 < N) __builtin_amdgcn_s_sleep(GAP);
        }
        bool ok = false;
#pragma unroll
        for (int i = 0; i < N; ++i) {
            if (__builtin_amdgcn_ballot_w64(on && (f[i][0] != X.e0 || f[i][1] != X.e1)) == 0) { ok = true; break; }
            if (cnt == 8) COUNT(8); else if (cnt == 4) COUNT(4); else COUNT(3);
        }
        if (ok) break;
        __builtin_amdgcn_s_sleep(BACKOFF);
    }
    CBAR();
}
DI void hint_set(const Ctx& X, int line, int word) {
    if (X.lane == 0) (void)__hip_atomic_exchange((GAS unsigned*)(X.wsp + OFF_AH + line * 128 + word * 4), X.e0, RLX_AGENT);
    CBAR();
}
DI void hint_wait(const Ctx& X, int line, int cnt) {
    GAS unsigned* p = (GAS unsigned*)(X.wsp + OFF_AH + line * 128) + (X.lane & 15);
    const unsigned zero = opaque(0u);
#pragma nounroll
    for (unsigned spins = 0; spins < SPIN_CAP; ++spins) {
        unsigned f = X.e0;
        if (X.lane < 16) f = __hip_atomic_fetch_add(p, zero, RLX_AGENT);
        if (__builtin_amdgcn_ballot_w64(f != X.e0) == 0) break;
        if (cnt == 8) COUNT(8); else COUNT(7);
        __builtin_amdgcn_s_sleep(BACKOFF);
    }
    CBAR();
}
template <class F> DI void rendezvous(const Ctx& X, int hop, F&& side) {
    if (OB_TIMED && (hop & 1)) {
        if (X.g == 0) __builtin_amdgcn_s_sleep(OB_TIMED); else side();
        CBAR();
        return;
    }
    if (X.u == 3 && X.g == ((hop & 1) ^ 1)) hint_set(X, 32 + hop * 16 + X.t, X.c);
    if (X.g == 0 || X.u < 3) { __builtin_amdgcn_s_sleep(DELAY_SIDE); side(); }
    else {
        if (hop & 1) __builtin_amdgcn_s_sleep(7);
        __builtin_amdgcn_s_sleep(DELAY_H);
        hint_wait(X, 32 + hop * 16 + X.t, 8);
        __builtin_amdgcn_s_sleep(DELAY_G);
    }
    bar();
}

template <int A, int B, int N> DI void ld_range(const Ctx& X, u32x4 (&xf)[N], unsigned base, unsigned rot) {
#pragma unroll
    for (int s = A; s < B; ++s) xf[s] = ldw(X, X.l16 + (s & 3) * 1024, base + ((s >> 2) ^ rot) * 4096);
}
DI unsigned krot(const Ctx& X, int mode) { return ROT && mode ? X.c & 7 : 0; }

DI unsigned tile_of(const Ctx& X) { return 4 * X.t + X.u; }
DI unsigned kv_so(const Ctx& X, int la) { return OFF_KV + la * STEP_KV + tile_of(X) * 65536 + X.c * 4096; }
DI unsigned fr_so(const Ctx& X, unsigned buf) { return buf + tile_of(X) * 32768 + X.c * 2048; }
DI unsigned st_so(const Ctx& X, int li) { return OFF_ST + li * STEP_ST + tile_of(X) * 4096 + X.c * 256; }
DI unsigned kvp_so(const Ctx& X, int lb) { return OFF_KVP + lb * SZ_KVP + (X.c * 16 + X.t) * 2304; }
DI void mark2(const Ctx& X, unsigned so) { const u32x4 m = splat4(MARK); stw(X, m, X.l16, so); stw(X, m, X.l16 + 1024, so); }

DI void mark_x(const Ctx& X, int li) {
    mark2(X, fr_so(X, OFF_X + li * STEP_XO));
    stw32(X, MARK, X.lane * 4, st_so(X, li));
}
DI void stats_finish(const Ctx& X, float s1, float s2, int tok) {
    const float mean = s1 * (1.f / 512.f);
    const float var = fmaxf(s2 * (1.f / 512.f) - mean * mean, 0.f);
    const unsigned h = pk2(-mean, -mean);
    const float lo = -mean - bflo(h);
    sts32f(L_STAT + X.u * 128 + tok * 4, __builtin_amdgcn_rsqf(var + 1e-5f));
    *(LAS u32x2*)(size_t)(L_MEAN + X.u * 256 + tok * 8) = u32x2{h, pk2(lo, lo)};
}
DI void stats_gather(const Ctx& X, int li, unsigned tag) {
    const unsigned sts = OFF_ST + li * STEP_ST + tile_of(X) * 4096;
    u32x4 st[4];
    __builtin_amdgcn_s_setprio(1);
#pragma nounroll
    for (unsigned spins = 0; spins < SPIN_CAP; ++spins) {
        CBAR();
#pragma unroll
        for (int i = 0; i < 4; ++i) st[i] = ldw(X, X.l16 + i * 1024, sts);
        if (!marked(max(umax8(st[0], st[1]), umax8(st[2], st[3])))) break;
        COUNT(7);
    }
    float v[4];
#pragma unroll
    for (int k = 0; k < 4; ++k) {
        v[k] = opaque(opaque(__uint_as_float(st[0][k]) + __uint_as_float(st[1][k])) + opaque(__uint_as_float(st[2][k]) + __uint_as_float(st[3][k])));
        auto r16 = __builtin_amdgcn_permlane16_swap(__float_as_uint(v[k]), __float_as_uint(v[k]), false, false);
        v[k] = xhalf_sum(__uint_as_float(r16[0]) + __uint_as_float(r16[1]));
    }
    const bool odd = X.lane & 16;
    stats_finish(X, odd ? v[2] : v[0], odd ? v[3] : v[1], (X.lane & 15) * 2 + (odd ? 1 : 0));
    lgkwait<0>(); CBAR();
    *(volatile LAS unsigned char*)(size_t)(L_SF + X.u) = (unsigned char)tag;
    CBAR();
}
DI void colsum_dma(const Ctx& X, int copy, unsigned area) {
    const unsigned so = OFF_CSP + copy * SZ_CSP + X.c * 32 * 384 + X.u * 128, vo = (X.lane >> 3) * 384 + (X.lane & 7) * 16;
#pragma unroll
    for (int i = 0; i < 4; ++i) dma1<0, 16>(X.ws, vo, so + i * 3072, area + i * 1024);
}
DI void colsum_frag(const Ctx& X, int mat, int tile, float s) {
    const unsigned hh = pk2(s, s) & 0xffffu;
    const unsigned w = X.hi ? 0u : (hh | (pk2(s - bflo(hh), 0.f) << 16));
    sts128(L_CSF + mat * 3072 + tile * 1024 + X.l16, u32x4{w, w, 0u, 0u});
}
DI float colsum_partials(const Ctx& X, unsigned area) {
    float s = 0.f;
#pragma unroll
    for (int i = 0; i < 16; ++i) s += lds32f(area + (X.hi * 16 + i) * 128 + (X.lane & 31) * 4);
    return xhalf_sum(s);
}
DI u32x4 tr_frag(unsigned a, unsigned step) {
    const u32x2 l2 = __builtin_bit_cast(u32x2, __builtin_amdgcn_ds_read_tr16_b64_v4i16((LAS s16x4*)(size_t)a));
    const u32x2 h2 = __builtin_bit_cast(u32x2, __builtin_amdgcn_ds_read_tr16_b64_v4i16((LAS s16x4*)(size_t)(a + step)));
    return u32x4{l2[0], l2[1], h2[0], h2[1]};
}
DI unsigned tr_lane64(const Ctx& X) { return (8 * X.hi + ((X.lane >> 2) & 3)) * 64 + ((X.lane >> 4) & 1) * 32 + (X.lane & 3) * 8; }
DI void colsum_add(u32x4 v, float& s0, float& s1) {
#pragma unroll
    for (int i = 0; i < 4; ++i) { s0 += bflo(v[i]); s1 += bfhi(v[i]); }
    PIN(s0); PIN(s1);
}
template <int WHICH> DI unsigned read_progress() {
    if constexpr (WHICH == 0) return *(volatile LAS unsigned*)(size_t)L_CNT;
    else if constexpr (WHICH == 1) return min(*(volatile LAS unsigned*)(size_t)(L_CNT + 4), *(volatile LAS unsigned*)(size_t)(L_CNT + 8));
    else return min(read_progress<0>(), read_progress<1>());
}
template <int WHICH> DI void wait_progress(unsigned v, unsigned need) {
    if (UNLIKELY(__builtin_amdgcn_ballot_w64(v < need) != 0)) {
#pragma nounroll
        for (unsigned spins = 0; spins < SPIN_CAP * 16 && __builtin_amdgcn_ballot_w64(read_progress<WHICH>() < need) != 0; ++spins) __builtin_amdgcn_s_sleep(1);
    }
    CBAR();
}
DI void set_progress(int slot, unsigned v) { CBAR(); *(volatile LAS unsigned*)(size_t)(L_CNT + slot * 4) = v; CBAR(); }
DI void wait_signal(unsigned v) {
#pragma nounroll
    for (unsigned spins = 0; spins < SPIN_CAP * 16 && (unsigned)__builtin_amdgcn_readfirstlane(*(volatile LAS unsigned*)(size_t)(L_CNT + 12)) < v; ++spins) __builtin_amdgcn_s_sleep(2);
    CBAR();
}
constexpr int oissued(int s) { int n = OD + OR * s; return n < 32 ? n : 32; }
constexpr int xissued(int s) { int n = XD + s + (s < XR ? s : XR); return n < 32 ? n : 32; }
DI void settle_x(const Ctx& X, unsigned xbase, int n = 8) {
#pragma nounroll
    for (int k = 0; k < n; ++k) {
#pragma nounroll
        for (unsigned spins = 0; spins < SPIN_CAP; ++spins) {
            CBAR();
            u32x4 v[4];
#pragma unroll
            for (int i = 0; i < 4; ++i) v[i] = ldw(X, X.l16 + i * 1024, xbase + k * 4096);
            if (!marked(max(umax8(v[0], v[1]), umax8(v[2], v[3])))) break;
        }
    }
    CBAR(); __builtin_amdgcn_s_waitcnt(0x0F70); CBAR();
}
template <int MODE>
DI void gemm_kv(Ctx& X, u32x4 (&xf)[32], int li, f32x16& a1, f32x16& a2, float& ra, f32x4 (&rn)[4], u32x4& mf) {
    const unsigned xbase = OFF_X + li * STEP_XO + tile_of(X) * 32768, rot = krot(X, MODE);
    if constexpr (MODE != 0) { ld_range<0, XD>(X, xf, xbase, rot); CBAR(); }
    a1 = zero16(); a2 = zero16();
    u32x4 w[3][2]; unsigned cf, sf;
    const unsigned wl = MODE == 0 ? opaque(tr_lane64(X)) : X.l16;
    auto wfrag = [&](unsigned a) { return MODE == 0 ? tr_frag(a, 256) : lds128(a); };
    unsigned o = opaque(L_W + wl + rot * 12288);
    const unsigned oc = opaque(L_CSF + (MODE == 2 ? 4096 : 1024) + X.l16);
    const unsigned orn = opaque(L_STAT + X.u * 128 + X.hi * 16), ora = opaque(L_STAT + X.u * 128 + (X.lane & 31) * 4), omf = opaque(L_MEAN + X.u * 256 + (X.lane & 31) * 8);
#pragma unroll
    for (int s = 0; s < 2; ++s)
#pragma unroll
        for (int i = 0; i < 2; ++i) w[s][i] = wfrag(o + s * 3072 + 1024 + i * 1024);
    static_for<0, 33>([&](auto S) {
        constexpr int s = S;
        auto wread = [&](int i) {
            if constexpr (s + 2 < 32) {
                if (((s + 2) & 3) == 0 && i == 0) o = opaque(L_W + wl + (((s + 2) >> 2) ^ rot) * 12288);
                w[(s + 2) % 3][i] = wfrag(o + ((s + 2) & 3) * 3072 + 1024 + i * 1024);
            }
            else if constexpr (s == 30) {
                w[(s + 2) % 3][i] = lds128(oc + i * 1024);
                if (i) ra = lds32f(ora); else { const u32x2 m = *(const LAS u32x2*)(size_t)omf; mf = u32x4{m[0], m[1], 0u, 0u}; }
            }
            else if constexpr (s == 31) { rn[2 * i] = lds128f(orn + i * 64); rn[2 * i + 1] = lds128f(orn + i * 64 + 32); }
        };
        if constexpr (s == 0 || s == 16) pad<(MODE == 0 ? 1 : MODE == 1 ? 28 : 22) + s / 16>();
        if constexpr (MODE == 0 && (s == 5 || s == 13 || s == 21 || s == 29)) cf = read_progress<1>();
        if constexpr (MODE == 0 && (s == 6 || s == 14 || s == 22 || s == 30)) wait_progress<1>(cf, s == 30 ? 33 : s + 10);
        if constexpr (MODE == 2 && s == 29) cf = read_progress<2>();
        if constexpr (MODE == 2 && s == 30) wait_progress<2>(cf, 35);
        if constexpr (MODE != 0 && s == 29) sf = *(volatile LAS unsigned char*)(size_t)(L_SF + X.u);
        if constexpr (MODE != 0 && s == 30) {
            if (UNLIKELY(__builtin_amdgcn_ballot_w64(sf != (unsigned)li + 1) != 0)) {
#pragma nounroll
                for (unsigned spins = 0; spins < SPIN_CAP * 16 && __builtin_amdgcn_ballot_w64(*(volatile LAS unsigned char*)(size_t)(L_SF + X.u) != (unsigned)li + 1) != 0; ++spins) __builtin_amdgcn_s_sleep(1);
            }
            CBAR();
        }
        if constexpr (MODE == 0 && s < 31) lgkwait<4>(); else if constexpr (s < 31) lgkwait<2>(); else lgkwait<4>();
        SBAR();
        const u32x4 xv = s < 32 ? xf[s & 31] : mf;
        if (MODE != 2) a1 = mfma(w[s % 3][0], xv, a1); else a1 = mfma(xv, w[s % 3][0], a1);
        PIN(a1);
        wread(0);
        if constexpr (MODE != 0) ld_range<xissued(s), xissued(s + 1)>(X, xf, xbase, rot);
        CBAR(); SBAR();
        a2 = mfma(xv, w[s % 3][1], a2);
        PIN(a2);
        wread(1);
        SBAR();
    });
}
template <int MODE>
DI void gemm_q(Ctx& X, const u32x4 (&xf)[32], u32x4 mf, f32x16& a0, int la) {
    a0 = zero16();
    u32x4 w[5]; unsigned cf;
    const unsigned rot = krot(X, MODE);
    const unsigned wl = MODE == 0 ? opaque(tr_lane64(X)) : X.l16;
    auto wfrag = [&](unsigned a) { return MODE == 0 ? tr_frag(a, 256) : lds128(a); };
    unsigned o = opaque(L_W + wl + rot * 12288);
    const unsigned oc = opaque(L_CSF + (MODE == 2 ? 3072 : 0) + X.l16);
    if constexpr (MODE == 0) wait_progress<0>(read_progress<0>(), 20);
    if constexpr (MODE == 1) wait_progress<2>(read_progress<2>(), 36);
#pragma unroll
    for (int s = 0; s < 4; ++s) w[s] = wfrag(o + s * 3072);
    static_for<0, 33>([&](auto S) {
        constexpr int s = S;
        if constexpr (s == 0 || s == 24) pad<(MODE == 0 ? 3 : MODE == 1 ? 30 : 24) + s / 24>();
        if constexpr (MODE == 0 && s == 14) cf = read_progress<0>();
        if constexpr (MODE == 0 && s == 16) wait_progress<0>(cf, 33);
        SBAR();
        a0 = mfma(w[s % 5], s < 32 ? xf[s & 31] : mf, a0);
        PIN(a0);
        if constexpr (s + 4 < 32) {
            if ((s & 3) == 0) o = opaque(L_W + wl + (((s + 4) >> 2) ^ rot) * 12288);
            w[(s + 4) % 5] = wfrag(o + (s & 3) * 3072);
        }
        else if constexpr (s == 28) w[(s + 4) % 5] = lds128(oc);
        if constexpr (MODE != 2 && s == KVFLAG_AT) {
            vmwait<0>();
            if (X.lane == 0) (void)__hip_atomic_exchange((gu64*)(X.wsp + OFF_KVFLAG + (la * 256 + X.t * 16 + X.c) * 128 + X.u * 8), (unsigned long long)X.e0 | ((unsigned long long)X.e1 << 32), RLX_AGENT);
            CBAR();
            if (X.u == 3) set_progress(3, 2 * la + 1);
        }
        SBAR();
    });
}
DI u32x4 pack8(const f32x16& a, int b) { u32x4 r = {pk2(a[b], a[b + 1]), pk2(a[b + 2], a[b + 3]), pk2(a[b + 4], a[b + 5]), pk2(a[b + 6], a[b + 7])}; return r; }
DI u32x4 pack4x2(f32x2 p0, f32x2 p1, f32x2 p2, f32x2 p3) {
    asm volatile("" : "+v"(p0), "+v"(p1), "+v"(p2), "+v"(p3));
    u32x4 r = {pk2(p0), pk2(p1), pk2(p2), pk2(p3)}; return r;
}
DI u32x4 pack8s(const f32x16& a, int b, float s) { return pack4x2(pair(a, b) * s, pair(a, b + 2) * s, pair(a, b + 4) * s, pair(a, b + 6) * s); }
DI u32x4 pack8m(const f32x16& a, float s) {
    float t[8];
#pragma unroll
    for (int i = 0; i < 8; ++i) t[i] = opaque(a[i] * s);
    u32x4 r = {pk2(t[0], t[1]), pk2(t[2], t[3]), pk2(t[4], t[5]), pk2(t[6], t[7])}; return r;
}
DI f32x2 rn2(const f32x4 (&rn)[4], int i) { f32x2 r = {rn[i >> 2][i & 3], rn[i >> 2][(i & 3) + 1]}; return r; }
DI u32x4 pack8r(const f32x16& a, int b, const f32x4 (&rn)[4]) {
    return pack4x2(pair(a, b) * rn2(rn, b), pair(a, b + 2) * rn2(rn, b + 2), pair(a, b + 4) * rn2(rn, b + 4), pair(a, b + 6) * rn2(rn, b + 6));
}

template <int MODE>
DI void front_a(Ctx& X, u32x4 (&xf)[32], int la) {
    f32x16 a0, a1, a2; f32x4 rn[4]; float ra; u32x4 mf;
#pragma nounroll
    for (int pass = 0;; ++pass) {
        gemm_kv<MODE>(X, xf, 1, a1, a2, ra, rn, mf);
        if (MODE == 0 || pass || !UNLIKELY(__builtin_amdgcn_ballot_w64(a1[0] != a1[0]) != 0)) break;
        COUNT(1);
        settle_x(X, OFF_X + STEP_XO + tile_of(X) * 32768);
    }
    STAMP(9 + la * 24);
    const unsigned so = kv_so(X, la), lb = L_QKV + X.u * 6144 + X.l16;
    const unsigned hb = (X.lane >> 4) & 1, ch = X.hi * 16 + (X.lane & 15);
    const unsigned lv = L_QKV + X.u * 6144 + hb * 3072 + ch * 32;
    const u32x4 k0 = pack8s(a1, 0, ra);
    stw(X, k0, X.l16, so); sts128(lb + 1024, k0); SBAR();
    const u32x4 k1 = pack8s(a1, 8, ra);
    stw(X, k1, X.l16 + 2048, so); sts128(lb + 4096, k1); SBAR();
    const u32x4 v0 = pack8r(a2, 0, rn);
    stw(X, v0, hb * 2048 + ch * 16 + 1024, so); sts128(lv + 2048, v0); SBAR();
    const u32x4 v1 = pack8r(a2, 8, rn);
    stw(X, v1, hb * 2048 + ch * 16 + 1536, so); sts128(lv + 2064, v1); SBAR();
    STAMP(30 + la * 24);
    gemm_q<MODE>(X, xf, mf, a0, la);
    const float rq = ra * QSCALE;
    sts128(lb, pack8s(a0, 0, rq)); SBAR();
    sts128(lb + 3072, pack8s(a0, 8, rq)); SBAR();
    f32x2 m0 = pair(a0, 0) * pair(a1, 0), m1 = pair(a0, 8) * pair(a1, 8);
#pragma unroll
    for (int j = 2; j < 8; j += 2) { m0 += pair(a0, j) * pair(a1, j); m1 += pair(a0, 8 + j) * pair(a1, 8 + j); }
    sts32f(L_MH + X.u * 256 + X.lane * 4, __uint_as_float(pk2(xhalf_sum2(m0[0] + m0[1], m1[0] + m1[1]) * -(ra * rq), 0.f)));
}

constexpr unsigned long long diag_mask(int r) {
    unsigned long long m = 0;
    for (int l = 0; l < 64; ++l) if ((r & 3) + 8 * (r >> 2) + 4 * (l >> 5) <= (l & 31)) m |= 1ull << l;
    return m;
}
struct KVt { u32x4 k, v0, v1; };
template <int I> DI unsigned pmask() {
    return (__builtin_amdgcn_inverse_ballot_w64(diag_mask(2 * I)) ? 0xffffu : 0u) | (__builtin_amdgcn_inverse_ballot_w64(diag_mask(2 * I + 1)) ? 0xffff0000u : 0u);
}
struct KVaddr { unsigned kb, vb, vs; int t4; };
DI KVaddr kv_addr(const Ctx& X, int la) {
    KVaddr a; a.t4 = 4 * X.t;
    a.kb = OFF_KV + la * STEP_KV + X.t * 262144 + (2 * X.c + (X.u & 1)) * 2048;
    const bool on = X.lane & 16;
    a.vb = opaque(on ? OFF_ONES + (X.t * 16 + X.c) * 1024 : a.kb + 1024 + (X.hi * 16 + (X.lane & 15)) * 16);
    a.vs = opaque(on ? 0u : 65536u);
    return a;
}
template <int PART> DI void kv_load(const Ctx& X, const KVaddr& a, KVt& r, int e) {
    const bool ok = a.t4 + e >= 0;
    if constexpr (PART & 1) r.k = ldw(X, X.l16, ok ? a.kb + e * 65536 : OOB);
    if constexpr (PART & 2) { const unsigned vo = a.vb + a.vs * e, so = ok ? 0u : OOB; r.v0 = ldw(X, vo, so); r.v1 = ldw(X, vo + 512, so); }
}
DI void att_slow(const Ctx& X, const KVaddr& a, int q0, u32x4 q, f32x16& o) {
    const int qi = X.lane & 31;
    float m = -INFINITY;
    o = zero16();
#pragma nounroll
    for (int pass = 0; pass < 2; ++pass) {
#pragma nounroll
        for (int d = 0; d <= 8 && a.t4 + q0 - d >= 0; ++d) {
            KVt kv;
            kv_load<3>(X, a, kv, q0 - d);
            f32x16 s = mfma(kv.k, q, zero16());
            float mm = -INFINITY;
#pragma unroll
            for (int r = 0; r < 16; ++r) {
                const int dist = qi - ((r & 3) + 8 * (r >> 2) + 4 * X.hi) + 32 * d;
                s[r] = (dist >= 0 && dist < 256) ? s[r] : -INFINITY;
                mm = fmaxf(mm, s[r]);
            }
            if (pass == 0) m = fmaxf(m, mm);
            else {
#pragma unroll
                for (int r = 0; r < 16; ++r) s[r] = __builtin_amdgcn_exp2f(s[r] - m);
                o = mfma(kv.v0, pack8(s, 0), o); o = mfma(kv.v1, pack8(s, 8), o);
            }
        }
        m = xhalf_max(m);
    }
}
struct Item { int unit, e, slot; bool merged; };
constexpr Item ITEMS[4][8] = {
    {{1, 0, 4, 0}, {1, 1, 5, 0}, {0, -1, 0, 0}, {1, -1, 0, 0}, {0, -3, 1, 0}, {1, -3, 1, 0}, {0, -6, 2, 0}, {0, -8, 3, 1}},
    {{1, 2, 4, 0}, {0, -2, 0, 0}, {1, -2, 0, 0}, {0, -4, 1, 0}, {1, -4, 1, 0}, {0, -5, 2, 0}, {1, -5, 2, 1}, {0, -7, 3, 0}},
    {{1, 0, 4, 0}, {0, 0, 4, 0}, {0, -1, 0, 0}, {1, -1, 0, 0}, {0, -3, 1, 0}, {1, -3, 1, 0}, {0, -6, 2, 0}, {1, -6, 2, 1}},
    {{1, 1, 4, 0}, {0, -2, 0, 0}, {1, -2, 0, 0}, {0, -4, 1, 0}, {1, -4, 1, 0}, {0, -5, 2, 0}, {1, -5, 2, 0}, {0, -7, 3, 1}}};
constexpr int slot_e(int v, int slot) { for (int i = 0; i < 8; ++i) if (ITEMS[v][i].slot == slot) return ITEMS[v][i].e; return 99; }
constexpr int merged_unit(int v) { for (int i = 0; i < 8; ++i) if (ITEMS[v][i].merged) return ITEMS[v][i].unit; return 0; }

template <int V>
DI void attention(Ctx& X, int la, const KVaddr& ka, u32x4& qk_, f32x16& res) {
    constexpr int QA = V >> 1, QB = 3 - QA, G = V & 1;
    constexpr int NOWN = G ? 1 : 2;
    u32x4 q[2]; f32x16 o[2];
    KVt kv[7];
    const unsigned qb = L_QKV + (X.u & 1) * 3072 + X.l16;
    const unsigned vb = (X.lane & 16) ? L_ONES : L_QKV + (X.u & 1) * 3072 + 2048 + (X.hi * 16 + (X.lane & 15)) * 32;
    const unsigned vs = (X.lane & 16) ? 0u : 6144u;
    const unsigned mh = L_MH + (X.u & 1) * 128 + (X.lane & 31) * 4;
    unsigned va[3] = {vb + vs * slot_e(V, 4), vb + vs * (slot_e(V, 5) != 99 ? slot_e(V, 5) : 0), vb + vs * (merged_unit(V) ? QB : QA)};
    unsigned ks[4], vo[4], vz[4];
    static_for<0, 4>([&](auto J) {
        constexpr int j = J, e = slot_e(V, j) != 99 ? slot_e(V, j) : -1;
        const bool ok = ka.t4 + e >= 0;
        ks[j] = sopaque(ok ? ka.kb + e * 65536 : OOB); vz[j] = sopaque(ok ? 0u : OOB); vo[j] = opaque(ka.vb + ka.vs * e);
    });
    const unsigned wg = sopaque(OFF_W + SZ_W + X.c * 98304 + (X.g * 4 + X.u) * 8192), wl = sopaque(L_W + (X.g * 4 + X.u) * 8192);
    const unsigned wg0 = sopaque(la == 0 ? OFF_WO + X.c * 32768 + (X.g * 4 + X.u) * 4096 : wg), wl0 = sopaque(la == 0 ? L_WO + (X.g * 4 + X.u) * 4096 : wl);
    auto wdma = [&](auto K) { if constexpr (K < 4) dma_lin<K, K + 1>(X, wg0, wl0); else dma_lin<K, K + 1>(X, wg, wl); };
    const u32x4 one0 = {opaque(X.hi ? 0u : 0x3f80u), 0u, 0u, 0u};
    asm volatile("" : "+v"(va[0]), "+v"(va[1]), "+v"(va[2]));
    if (G) __builtin_amdgcn_s_setprio(ATT_PRIO);
    bar();
    STAMP(11 + la * 24);
    auto own = [&](int slot, int e) { kv[slot].k = lds128(qb + e * 6144 + 1024); kv[slot].v0 = lds128(va[slot - 4]); kv[slot].v1 = lds128(va[slot - 4] + 16); };
    auto halo = [&](int j) { kv[j].k = ldw(X, X.l16, ks[j]); kv[j].v0 = ldw(X, vo[j], vz[j]); kv[j].v1 = ldw(X, vo[j] + 512, vz[j]); };
    if (HALO0 < 0) { halo(0); CBAR(); }
    const unsigned mb = __float_as_uint(lds32f(mh + QB * 256)), ma = __float_as_uint(lds32f(mh + QA * 256));
    q[1] = lds128(qb + QB * 6144);
    own(4, slot_e(V, 4));
    q[0] = lds128(qb + QA * 6144);
    if constexpr (slot_e(V, 5) != 99) own(5, slot_e(V, 5));
    own(6, merged_unit(V) ? QB : QA);
    SBAR();
    f32x16 nm[2], s[2], sx;
    o[0] = zero16(); o[1] = zero16();
    nm[1] = mfma(one0, u32x4{mb, 0u, 0u, 0u}, zero16()); nm[0] = mfma(one0, u32x4{ma, 0u, 0u, 0u}, zero16());
    u32x4 p0, p1; unsigned pub;
    auto qk = [&](auto N) {
        constexpr int n = N; constexpr Item it = ITEMS[V][n];
        s[n & 1] = mfma(kv[it.slot].k, q[it.unit], nm[it.unit]);
        PIN(s[n & 1]);
        if constexpr (it.merged) { sx = mfma(kv[6].k, q[it.unit], nm[it.unit]); PIN(sx); }
    };
    auto pv = [&](auto N, auto H) {
        constexpr Item it = ITEMS[V][N]; constexpr int hh = H;
        const u32x4 p = hh ? p1 : p0;
        if constexpr (it.merged) {
            const u32x4 m = {pmask<4 * hh>(), pmask<4 * hh + 1>(), pmask<4 * hh + 2>(), pmask<4 * hh + 3>()};
            const u32x4 d = p & m;
            o[it.unit] = mfma(hh ? kv[6].v1 : kv[6].v0, d, o[it.unit]);
            o[it.unit] = mfma(hh ? kv[it.slot].v1 : kv[it.slot].v0, p ^ d, o[it.unit]);
        } else o[it.unit] = mfma(hh ? kv[it.slot].v1 : kv[it.slot].v0, p, o[it.unit]);
        PIN(o[it.unit]);
    };
    u32x2 pubf = {0u, 0u};
    const unsigned pubo = X.hi ? ((X.c >> 1) + (X.lane & 31) * 8) * 128u : X.lane < 16 ? (X.t * 16 + X.lane) * 128u : OOB;
    if (V == 3 && la == 0 && X.u == 3) pubf = __builtin_amdgcn_raw_buffer_load_b64(X.ws, pubo, OFF_WFLAG, 16);
    qk(IC<0>{});
    SBAR();
    static_for<0, 8>([&](auto N) {
        constexpr int n = N; constexpr Item it = ITEMS[V][n];
        f32x16& c = s[n & 1];
        if constexpr (n == 2) STAMP(28 + la * 24);
        if constexpr (n == 5) STAMP(29 + la * 24);
        if constexpr (G && n == ATT_SWITCH) __builtin_amdgcn_s_setprio(0);
        if constexpr (V == 3 && n == 2) {
            if (la == 0 && X.u == 3) {
                if (__builtin_amdgcn_ballot_w64(pubo < OOB && (pubf[0] != X.e0 || pubf[1] != X.e1)) != 0) wait_word(X, pubo, OFF_WFLAG, 3);
                set_progress(3, 2);
            }
        }
        if constexpr (n == 3) pub = *(volatile LAS unsigned*)(size_t)(L_CNT + 12);
        if constexpr (n == 4) { if (UNLIKELY(__builtin_amdgcn_ballot_w64(pub < 2) != 0)) wait_signal(2); }
        if constexpr (n == 0 || n == 5) pad<(V < 4 ? 10 + 2 * V : 0) + n / 5>();
        constexpr bool late = n + 1 == NOWN;
        if constexpr (it.merged) static_for<0, 16>([&](auto R) { constexpr int r = R; c[r] = __builtin_amdgcn_inverse_ballot_w64(diag_mask(r)) ? sx[r] : c[r]; });
        if constexpr (n + 1 < 8 && !late) qk(IC<n + 1>{});
        if constexpr (n >= 4) wdma(IC<(n - 4) * 2>{});
        CBAR(); SBAR();
        float e[16];
#pragma unroll
        for (int i = 0; i < 5; ++i) e[i] = opaque(__builtin_amdgcn_exp2f(c[i]));
        SBAR();
        if constexpr (n == 0) { bar(); if (HALO0 == 0) halo(0); }
        if constexpr (n > 0) pv(IC<n - 1>{}, IC<1>{});
        if constexpr (n == (G ? 0 : 1)) halo(1);
        if constexpr (n == (G ? 2 : 3)) halo(2);
        if constexpr (n == (G ? 3 : 4) && slot_e(V, 3) != 99) halo(3);
        CBAR(); SBAR();
#pragma unroll
        for (int i = 5; i < 10; ++i) e[i] = __builtin_amdgcn_exp2f(c[i]);
        p0 = u32x4{pk2(e[0], e[1]), pk2(e[2], e[3]), pk2(e[4], e[5]), pk2(e[6], e[7])};
        PIN(e[8]); PIN(e[9]);
        SBAR();
        pv(N, IC<0>{});
        if constexpr (n >= 4) wdma(IC<(n - 4) * 2 + 1>{});
        CBAR(); SBAR();
#pragma unroll
        for (int i = 10; i < 16; ++i) e[i] = __builtin_amdgcn_exp2f(c[i]);
        p1 = u32x4{pk2(e[8], e[9]), pk2(e[10], e[11]), pk2(e[12], e[13]), pk2(e[14], e[15])};
        PIN(p1);
        if constexpr (n == 0 && HALO0 == 1) halo(0);
        CBAR();
        if constexpr (late) qk(IC<n + 1>{});
        SBAR();
    });
    pv(IC<7>{}, IC<1>{});
    STAMP(31 + la * 24);
    const unsigned ex = L_EX + X.u * 6144 + X.l16, ex1 = L_EX + X.u * 6144 + 2048 + X.lane * 4;
    const f32x16& sd = o[G ^ 1];
    sts128f(ex + G * 2304, f32x4{sd[0], sd[1], sd[2], sd[3]}); sts128f(ex + G * 2304 + 1024, f32x4{sd[4], sd[5], sd[6], sd[7]});
    sts32f(ex1 + G * 2304, sd[8]);
    vmwait<4>();

    bar();
    const f32x4 r0 = lds128f(ex + (G ^ 1) * 2304), r1 = lds128f(ex + (G ^ 1) * 2304 + 1024);
    const float r2 = lds32f(ex1 + (G ^ 1) * 2304);
#pragma unroll
    for (int i = 0; i < 4; ++i) { res[i] = o[G][i] + r0[i]; res[4 + i] = o[G][4 + i] + r1[i]; }
    res[8] = o[G][8] + r2;
    qk_ = q[G];
}

DI void outproj_finish(Ctx& X, const f32x16& acc, const u32x4 (&xo)[2], int li, bool last, unsigned char* out) {
    u32x4 xr[2];
    const __amdgpu_buffer_rsrc_t ro = last ? __builtin_amdgcn_make_buffer_rsrc(out, 0, MB2, 0x00020000) : X.ws;
    const unsigned vo = last ? (X.lane & 31) * 1024 + X.hi * 32 : X.l16, so = last ? tile_of(X) * 32768 + X.c * 64 : fr_so(X, OFF_X + li * STEP_XO), st = last ? 16 : 1024;
#pragma unroll
    for (int h = 0; h < 2; ++h) {
        const u32x4 a = pack8(acc, 8 * h);
#pragma unroll
        for (int i = 0; i < 4; ++i) xr[h][i] = min(pk2(bflo(xo[h][i]) + bflo(a[i]), bfhi(xo[h][i]) + bfhi(a[i])), MARK - 1u);
        __builtin_amdgcn_raw_buffer_store_b128(xr[h], ro, vo, so + h * st, 16);
        asm volatile("s_nop 1" :: "v"(xr[h]));
        SBAR();
    }
    if (!last) {
        float t1 = 0.f, t2 = 0.f, t3 = 0.f, t4 = 0.f;
#pragma unroll
        for (int i = 0; i < 4; ++i) {
            const unsigned n0 = xr[0][i], n1 = xr[1][i];
            const bf16x2 v0 = __builtin_bit_cast(bf16x2, n0), v1 = __builtin_bit_cast(bf16x2, n1);
            t1 = __builtin_amdgcn_fdot2_f32_bf16(v0, __builtin_bit_cast(bf16x2, ONE2), t1, false); t2 = __builtin_amdgcn_fdot2_f32_bf16(v0, v0, t2, false);
            t3 = __builtin_amdgcn_fdot2_f32_bf16(v1, __builtin_bit_cast(bf16x2, ONE2), t3, false); t4 = __builtin_amdgcn_fdot2_f32_bf16(v1, v1, t4, false);
        }
        stw32(X, __float_as_uint(xhalf_sum2(t1 + t3, t2 + t4)), (X.lane & 31) * 8 + X.hi * 4, st_so(X, li));
    }
    CBAR();
}
DI float phi1(float a, float r) {
    const float x = fmaf(a, r, 1e-6f);
    return fmaxf(x, 1e-6f) + __builtin_amdgcn_fmed3f(__builtin_amdgcn_exp2f(x * LOG2E), 0.f, 1.f);
}
DI unsigned phi2(f32x2 a, f32x2 r) {
    const f32x2 x = __builtin_elementwise_fma(a, r, f32x2{1e-6f, 1e-6f}), y = x * LOG2E;
    const f32x2 e = {__builtin_amdgcn_fmed3f(__builtin_amdgcn_exp2f(y[0]), 0.f, 1.f), __builtin_amdgcn_fmed3f(__builtin_amdgcn_exp2f(y[1]), 0.f, 1.f)};
    const f32x2 m = {fmaxf(x[0], 1e-6f), fmaxf(x[1], 1e-6f)};
    return pk2(m + e);
}

__global__ __launch_bounds__(512, 2) void fused_kernel(const unsigned short* __restrict__ xin, const unsigned short* __restrict__ wA, const unsigned short* __restrict__ woA,
                                                        const unsigned short* __restrict__ wB, const unsigned short* __restrict__ woB,
                                                        unsigned char* __restrict__ out, unsigned char* ws) {
    Ctx X;
    const unsigned warm = *(const GAS unsigned*)((const unsigned char*)__builtin_amdgcn_s_getpc() + ((threadIdx.x >> 6) * CODEWARM + min(threadIdx.x & 63u, CODEWARM - 1u)) * 128);
    { const int w = __builtin_amdgcn_readfirstlane(threadIdx.x >> 6); X.u0 = w & 3; X.g0 = w >> 2; }
    STAMP(0);
    X.ws = __builtin_amdgcn_make_buffer_rsrc(ws, 0, WS_BYTES, 0x00020000);
    X.wsp = ws;
    {
        const int b = blockIdx.x, i = b >> 3; X.x0 = b & 7;
        X.t0 = (X.x0 >> 1) * 4 + (i >> 3); X.c0 = (X.x0 & 1) * 8 + (i & 7);
    }
    fresh(X);
    const int cu = X.t * 16 + X.c;
    const u32x2 gen = __builtin_amdgcn_raw_buffer_load_b64(X.ws, 0, 0, 16);

    auto set_epoch = [&]() {
        asm volatile("" :: "v"(warm));
        const unsigned long long e = (__builtin_amdgcn_readfirstlane(gen[0]) | ((unsigned long long)__builtin_amdgcn_readfirstlane(gen[1]) << 32)) + 1;
        X.e0 = (unsigned)e; X.e1 = (unsigned)(e >> 32);
    };
    auto kv_ready = [&](int la) {
        wait_signal(2 * la + 1);
        __builtin_amdgcn_s_sleep(DELAY_KV);
        const int tt = X.t - 1 - (X.lane >> 2);
        const bool on = X.lane < 8 && tt >= 0;
        gu64* p = (gu64*)(ws + OFF_KVFLAG + (la * 256 + (on ? tt : 0) * 16 + X.c) * 128 + (X.lane & 3) * 8);
        const unsigned long long e = (unsigned long long)X.e0 | ((unsigned long long)X.e1 << 32), zero = opaque(0u);
#pragma nounroll
        for (unsigned spins = 0; spins < SPIN_CAP; ++spins) {
            unsigned long long f = e;
            if (on) f = __hip_atomic_fetch_add(p, zero, RLX_AGENT);
            if (__builtin_amdgcn_ballot_w64(f != e) == 0) break;
            COUNT(4);
            __builtin_amdgcn_s_sleep(BACKOFF);
        }
        CBAR();
    };
    auto x_read = [&]() {
#pragma nounroll
            for (unsigned spins = 0; spins < SPIN_CAP * 16 && (__builtin_amdgcn_readfirstlane(*(volatile LAS unsigned*)(size_t)L_XR) & 15) < 4; ++spins) __builtin_amdgcn_s_sleep(2);
            CBAR();
        };
    if (X.g == 0) {
        const __amdgpu_buffer_rsrc_t rx = __builtin_amdgcn_make_buffer_rsrc((void*)xin, 0, MB2, 0x00020000);
        const unsigned xs = tile_of(X) * 32768, xl = L_XI + X.u * 32768;
#pragma unroll
        for (int r = 0; r < 32; ++r) {
            const unsigned rr = r ^ ((X.c & 15) * 2);
            dma1<0, 0>(rx, X.l16 ^ ((rr & 15) * 16), xs + rr * 1024, xl + rr * 1024);
            if (r == 3) bar();
        }
        STAMP(1);
        vmwait<0>();
        set_epoch();
        STAMP(2);
        u32x4 xf[32];
        const int rho = X.lane & 31;
        const unsigned xb = xl + rho * 1024 + ((X.hi * 2) ^ (rho & 15)) * 16;
#pragma unroll
        for (int s = 0; s < 32; ++s) xf[s] = lds128((xb ^ ((((s >> 1) & 3) * 4 + (s & 1)) * 16)) + (s >> 3) * 256);
        lgkwait<0>(); CBAR();
        if (X.lane == 0) __hip_atomic_fetch_add((LAS unsigned*)(size_t)L_XR, 1u, __ATOMIC_RELAXED, __HIP_MEMORY_SCOPE_WORKGROUP);
        STAMP(3);
        pad<0>();
        float s1 = 0.f, s2 = 0.f;
#pragma unroll
        for (int s = 0; s < 32; ++s)
#pragma unroll
            for (int i = 0; i < 4; ++i) {
                const unsigned xw = xf[s][i];
                const bf16x2 v = __builtin_bit_cast(bf16x2, xw);
                s1 = __builtin_amdgcn_fdot2_f32_bf16(v, __builtin_bit_cast(bf16x2, ONE2), s1, false);
                s2 = __builtin_amdgcn_fdot2_f32_bf16(v, v, s2, false);
            }
        x_read();
        stats_finish(X, xhalf_sum(s1), xhalf_sum(s2), rho);
        STAMP(4);
        wait_progress<1>(read_progress<1>(), 8);
        STAMP(8);
        front_a<0>(X, xf, 0);
    } else {
        __builtin_amdgcn_s_setprio(1);
        const unsigned fo = ((X.c >> 1) + (X.lane & 31) * 8) * 128;
        if (X.u < 3) {
            const __amdgpu_buffer_rsrc_t rw = __builtin_amdgcn_make_buffer_rsrc((void*)wA, 0, SZ_W, 0x00020000);
            const unsigned tr = opaque(L_W + tr_lane64(X));
            const int gi = (X.t & 3) * 8 + (X.c & 7);
#pragma unroll
            for (int r = 0; r < 8; ++r) {
                dma1<0, 0>(rw, X.hi * 3072 + (X.lane & 31) * 16, (16 * gi + 2 * r) * 3072 + X.u * 1024 + (X.c >> 3) * 512, X.u * 8192 + r * 1024);
                if (r == 1) { set_progress(X.u, 0); bar(); }
            }
            STAMP(1);
            vmwait<0>();
            {
                f32x4 cs = {0.f, 0.f, 0.f, 0.f};
#pragma unroll
                for (int r = 0; r < 16; ++r) { const u32x2 v = *(const LAS u32x2*)(size_t)(X.u * 8192 + r * 512 + X.lane * 8); cs += f32x4{bflo(v[0]), bfhi(v[0]), bflo(v[1]), bfhi(v[1])}; }
                stw(X, __builtin_bit_cast(u32x4, cs), (X.lane >> 3) * 32 * 384 + (X.lane & 7) * 16, OFF_CSP + (1 + (X.t >> 2)) * SZ_CSP + ((X.c & 8) * 32 + gi) * 384 + X.u * 128);
            }
            set_epoch();
            lgkwait<0>(); CBAR();
            if (X.lane == 0) __hip_atomic_fetch_add((LAS unsigned*)(size_t)L_XR, 16u, __ATOMIC_RELAXED, __HIP_MEMORY_SCOPE_WORKGROUP);
            STAMP(2);
            vmwait<0>();
            flag_store(X, OFF_WFLAG + 65536 + cu * 512 + X.u * 128);
            const unsigned wv = ((X.lane >> 2) & 7) * 3072 + X.hi * 16 * 3072 + (X.lane & 3) * 16, wsb = X.c * 64 + X.u * 1024;
            auto piece = [&](auto S) { constexpr int s = S; dma1<0, 0>(rw, wv, wsb + (32 * (s >> 1) + 8 * (s & 1)) * 3072, L_W + X.u * 1024 + s * 3072); };
            if (X.u) {
#pragma nounroll
                for (unsigned spins = 0; spins < SPIN_CAP * 16 && __builtin_amdgcn_readfirstlane(*(volatile LAS unsigned*)(size_t)L_XR) < 48; ++spins) __builtin_amdgcn_s_sleep(1);
                CBAR();
                __builtin_amdgcn_s_sleep(DELAY_W);
                static_for<0, 10>(piece);
            }
            x_read();
            STAMP(5);
            const int src = (X.t & ~3) * 16 + ((X.lane >> 3) & 3) * 16 + (X.c & 8) + (X.lane & 7);
            const unsigned cfo = X.hi ? OOB : (src * 4 + X.u) * 128u;
            if (X.u == 0) { wait_word(X, cfo, OFF_WFLAG + 65536, 3); static_for<0, 10>(piece); }
            else { vmwait<0>(); set_progress(X.u, 10); }
            const u32x2 cf0 = __builtin_amdgcn_raw_buffer_load_b64(X.ws, cfo, OFF_WFLAG + 65536, 16);
            static_for<10, 24>(piece);
            if (__builtin_amdgcn_ballot_w64(!X.hi && (cf0[0] != X.e0 || cf0[1] != X.e1)) != 0) wait_word(X, cfo, OFF_WFLAG + 65536, 3);
            colsum_dma(X, 1 + (X.t >> 2), L_WO + X.u * 4096);
            vmwait<12>(); set_progress(X.u, 16);
            static_for<24, 32>(piece);
            vmwait<12>(); set_progress(X.u, 24);
            vmwait<8>();
            colsum_frag(X, 0, X.u, colsum_partials(X, L_WO + X.u * 4096));
            vmwait<0>();
            lgkwait<0>();
            set_progress(X.u, 33);
            STAMP(7);
            STAMP(4);
            wait_signal(1);
            stw(X, tr_frag(tr + X.t * 6144 + X.u * 1024, 256), X.l16, OFF_W + X.c * 98304 + X.t * 6144 + X.u * 1024);
            stw(X, tr_frag(tr + X.t * 6144 + X.u * 1024 + 3072, 256), X.l16 + 3072, OFF_W + X.c * 98304 + X.t * 6144 + X.u * 1024);
            mark_x(X, 0); mark2(X, fr_so(X, OFF_O + STEP_XO));
            if (X.u == 2) { X.u = 3; mark_x(X, 0); mark2(X, fr_so(X, OFF_O + STEP_XO)); }
        } else {
            const int ps = cu >> 3, pg = cu & 7;
            const __amdgpu_buffer_rsrc_t rb = __builtin_amdgcn_make_buffer_rsrc((void*)wB, 0, SZ_W, 0x00020000);
            const __amdgpu_buffer_rsrc_t roa = __builtin_amdgcn_make_buffer_rsrc((void*)woA, 0, SZ_WO, 0x00020000);
            const __amdgpu_buffer_rsrc_t rob = __builtin_amdgcn_make_buffer_rsrc((void*)woB, 0, SZ_WO, 0x00020000);
            const unsigned area = L_WO + 16384, ov = (X.lane >> 3) * 1024 + (X.lane & 7) * 16;
            const unsigned wv = (X.lane >> 3) * 3072 + (X.lane & 7) * 16, wk = (32 * (ps >> 1) + 8 * (ps & 1)) * 3072 + pg * 128;
            const int b = (X.lane >> 4) & 1, q = (X.lane >> 2) & 3, p = X.lane & 3;
            set_progress(3, 0); set_progress(4, 0); set_progress(5, 0);
            bar();
            set_epoch();
            hop_through<0, 5>();
            hop_through<10, 32>();
            x_read();
#pragma unroll
            for (int h = 0; h < 2; ++h) {
#pragma unroll
                for (int part = 0; part < 3; ++part) dma1<0, 0>(rb, wv, wk + part * 1024 + h * 16 * 3072, area + part * 2048 + h * 1024);
                dma1<0, 0>(roa, ov, (16 * ps + 8 * h) * 1024 + pg * 128, area + 6144 + h * 1024);
                dma1<0, 0>(rob, ov, (16 * ps + 8 * h) * 1024 + pg * 128, area + 8192 + h * 1024);
            }
            if (X.lane < 2) sts128(L_ONES + X.l16, splat4(ONE2));
            vmwait<0>();
            __builtin_amdgcn_s_sleep(DELAY_R);
#pragma unroll
            for (int i = 0; i < 4; ++i)
                stw(X, tr_frag(area + 6144 + (i >> 1) * 2048 + (4 * X.hi + q) * 128 + (16 * (p & 1) + 8 * b + 4 * (p >> 1)) * 2 + (i & 1) * 64, 1024), X.l16,
                    OFF_WO + (i >> 1) * SZ_WO + ((2 * pg + (i & 1)) * 32 + ps) * 1024);
#pragma unroll
            for (int part = 0; part < 3; ++part)
#pragma unroll
                for (int cc = 0; cc < 2; ++cc) {
                    const u32x4 v = tr_frag(area + part * 2048 + (8 * X.hi + q) * 128 + b * 32 + p * 8 + cc * 64, 512);
                    stw(X, v, X.l16 + part * 1024, OFF_W + SZ_W + ((2 * pg + cc) * 96 + ps * 3) * 1024);
                    float c0 = 0.f, c1 = 0.f;
                    colsum_add(v, c0, c1);
                    stw32(X, __float_as_uint(xhalf_sum(c0 + c1)), X.lane < 32 ? X.lane * 4u : OOB, OFF_CSP + ((2 * pg + cc) * 32 + ps) * 384 + part * 128);
                }
#pragma unroll
            for (int lb = 0; lb < 2; ++lb) {
                mark2(X, kvp_so(X, lb));
                stw(X, splat4(MARK), X.lane < 8 ? X.l16 + 2048 : OOB, kvp_so(X, lb));
            }
            stw(X, splat4(ONE2), X.lane < 2 ? X.lane * 512 : OOB, OFF_ONES + cu * 1024);
#pragma unroll
            for (int u = 0; u < 4; ++u) { X.u = u; mark2(X, fr_so(X, OFF_O)); }
            vmwait<0>();
            flag_store(X, OFF_WFLAG + cu * 128);
            STAMPU(6, 3);
            kv_ready(0);
        }
        STAMP(9);
    }

    for (int l = 0;; ++l) {
        const int it = l >> 1, sb = 9 + it * 24; (void)sb;
        fresh(X);
        if (!(l & 1)) {
            STAMP(sb + 1);
            const KVaddr ka = kv_addr(X, it);
            u32x4 q; f32x16 k;
            const int qt = X.g ? 3 - (X.u >> 1) : (X.u >> 1);
            const unsigned oso = OFF_O + l * STEP_XO + (4 * X.t + qt) * 32768 + (2 * X.c + (X.u & 1)) * 1024;
            if (X.u < 2) { if (X.g == 0) attention<0>(X, it, ka, q, k); else attention<1>(X, it, ka, q, k); }
            else { if (X.g == 0) attention<2>(X, it, ka, q, k); else attention<3>(X, it, ka, q, k); }
            pad<18>();
            const float sa = (fabsf(k[0]) + fabsf(k[1])) + (fabsf(k[2]) + fabsf(k[3])) + (fabsf(k[4]) + fabsf(k[5])) + (fabsf(k[6]) + fabsf(k[7]));
            if (UNLIKELY(__builtin_amdgcn_ballot_w64(!(sa < 0x1p120f) || !(k[8] < 0x1p100f) || !(k[8] > 0x1p-20f)) != 0)) { COUNT(5); att_slow(X, ka, qt, q, k); }
            stw(X, pack8s(k, 0, __builtin_amdgcn_rcpf(k[8])), X.l16, oso);
            STAMP(sb + 3);
        } else if (X.g == 0) {
            u32x4 xf[32];
            STAMP(sb + 6);
            f32x16 a0, kv, zz; float ra; u32x4 mf;
#pragma nounroll
            for (int pass = 0;; ++pass) {
                f32x16 a1, a2; f32x4 rn[4];
                gemm_kv<2>(X, xf, l - 1, a1, a2, ra, rn, mf);
                STAMP(sb + 7);
                u32x4 k0, k1;
#pragma unroll
                for (int i = 0; i < 4; ++i) k0[i] = phi2(pair(a1, 2 * i), rn2(rn, 2 * i));
#pragma unroll
                for (int i = 0; i < 4; ++i) k1[i] = phi2(pair(a1, 8 + 2 * i), rn2(rn, 8 + 2 * i));
                const u32x4 v0 = pack8r(a2, 0, rn), v1 = pack8r(a2, 8, rn);
                SBAR();
                kv = mfma(k0, v0, zero16());
                kv = mfma(k1, v1, kv);
                zz = mfma(k0, splat4(ONE2), zero16());
                zz = mfma(k1, splat4(ONE2), zz);
                PIN(kv); PIN(zz);
                if (pass || !UNLIKELY(__builtin_amdgcn_ballot_w64(kv[0] != kv[0]) != 0)) break;
                COUNT(2);
                settle_x(X, OFF_X + (l - 1) * STEP_XO + tile_of(X) * 32768);
            }
            pad<26>();
            const bool hbit = X.lane & 16;
            const unsigned rb = L_RED + X.u * 6144 + X.lane * 32, rz = (X.lane & 15) == 0 ? L_RED + X.u * 6144 + 2048 + X.lane * 2 : L_RED + X.u * 6144 + 4096 + X.lane * 32;
            f32x4 e0, e1, z0, z1;
#pragma unroll
            for (int j = 0; j < 4; ++j) { const float p0 = opaque(kv[8 + j]), p1 = opaque(kv[12 + j]); e0[j] = hbit ? p0 : kv[j]; e1[j] = hbit ? p1 : kv[4 + j]; }
            sts128f(rb, e0); sts128f(rb + 16, e1);
#pragma unroll
            for (int j = 0; j < 4; ++j) { const float p2 = opaque(zz[8 + j]), p3 = opaque(zz[12 + j]); z0[j] = hbit ? p2 : zz[j]; z1[j] = hbit ? p3 : zz[4 + j]; }
            sts128f(rz, z0); sts128f(rz + 16, z1);
            STAMP(sb + 8);
            bar();
            gemm_q<2>(X, xf, mf, a0, 0);
            u32x4 p0, p1;
#pragma unroll
            for (int i = 0; i < 4; ++i) { p0[i] = phi2(pair(a0, 2 * i), f32x2{ra, ra}); p1[i] = phi2(pair(a0, 8 + 2 * i), f32x2{ra, ra}); }
            PIN(p0); PIN(p1);
            const unsigned ko = opaque(L_KVF + (hbit ? 512 + X.hi * 16 : (X.hi * 16 + (X.lane & 15)) * 16)), po = sopaque(fr_so(X, OFF_O + l * STEP_XO));
            STAMP(sb + 10);
            bar();
            STAMP(sb + 12);
            const u32x4 f0 = lds128(ko), f1 = lds128(ko + 544);
            const f32x16 n0 = mfma(f0, p0, zero16()), n1 = mfma(f1, p1, zero16());
            stw(X, pack8s(n0, 0, __builtin_amdgcn_rcpf(n0[8] + 1e-6f)), X.l16, po);
            stw(X, pack8s(n1, 0, __builtin_amdgcn_rcpf(n1[8] + 1e-6f)), X.l16 + 1024, po);
            STAMP(sb + 15);
        } else {
            if (it == 0 && X.u < 3) {
                colsum_dma(X, 0, L_WO + X.u * 4096);
                vmwait<0>();
                colsum_frag(X, 1, X.u, colsum_partials(X, L_WO + X.u * 4096));
                lgkwait<0>();
                set_progress(X.u, 35);
                __builtin_amdgcn_s_sleep(DELAY_ST > 14 ? DELAY_ST - 14 : 0);
            } else __builtin_amdgcn_s_sleep(DELAY_ST);
            stats_gather(X, l - 1, l);
            if (it == 0) { __builtin_amdgcn_s_sleep(DELAY_MK); mark_x(X, 1); mark2(X, fr_so(X, OFF_O + 2 * STEP_XO)); }
            if (it == 0 && X.u == 3) {
                wait_word(X, X.lane < 32 ? ((X.lane & 15) * 16 + X.c) * 128u + (X.lane >> 4) * 32768u : OOB, OFF_WFLAG, 3);
            }
            pad<21>();
            __builtin_amdgcn_s_setprio(2);
            const unsigned fs = sopaque(OFF_KVP + it * SZ_KVP + X.c * 16 * 2304);
            const unsigned o = opaque(L_RED + X.u * 1024 + X.l16), so = sopaque(kvp_so(X, it)), zo = opaque(X.u == 0 && X.lane < 8 ? X.l16 + 2048 : OOB);
            const unsigned zv = opaque((X.lane >> 3) * 2304u + 2048 + (X.lane & 7) * 16);
            bar();
            if (X.u < 2) {
                f32x4 r[8];
#pragma unroll
                for (int i = 0; i < 8; ++i) r[i] = lds128f(o + (i & 3) * 6144 + (i >> 2) * 2048);
                SBAR();
                f32x4 s, z;
#pragma unroll
                for (int i = 0; i < 4; ++i) s[i] = opaque(opaque(r[0][i] + r[1][i]) + opaque(r[2][i] + r[3][i]));
                stw(X, __builtin_bit_cast(u32x4, s), X.l16 + X.u * 1024, so);
                SBAR();
#pragma unroll
                for (int i = 0; i < 4; ++i) z[i] = opaque(opaque(r[4][i] + r[5][i]) + opaque(r[6][i] + r[7][i]));
                stw(X, __builtin_bit_cast(u32x4, z), zo, so);
                CBAR();
                if (X.u == 0) hint_set(X, it * 16 + X.c, X.t);
                STAMP(sb + 9);
                dma_lin<0, 16>(X, OFF_WO + SZ_WO + X.c * 32768 + X.u * 16384, L_WO + X.u * 16384);
            }
            if (X.u == 3) {
                __builtin_amdgcn_s_sleep(DELAY_KVP);
                hint_wait(X, it * 16 + X.c, 7);
                __builtin_amdgcn_s_sleep(DELAY_KVQ);
                set_progress(4, 1000 + it);
            } else if (X.u) {
#pragma nounroll
                for (unsigned spins = 0; spins < SPIN_CAP * 16 && __builtin_amdgcn_readfirstlane(*(volatile LAS unsigned*)(size_t)L_XR) != 1000 + it; ++spins) __builtin_amdgcn_s_sleep(1);
                CBAR();
            }
            if (X.u < 2) {
                if (X.u == 1) {
                    CBAR();
                    u32x4 p0 = ldw(X, zv, fs), p1 = ldw(X, zv, fs + 8 * 2304);
#pragma nounroll
                    for (unsigned spins = 0; spins < SPIN_CAP; ++spins) {
                        const bool miss = umax8(p0, p1) == MARK;
                        if (__builtin_amdgcn_ballot_w64(miss) == 0) break;
                        __builtin_amdgcn_s_sleep(KVP_BACKOFF);
                        CBAR();
                        if (miss) { p0 = ldw(X, zv, fs); p1 = ldw(X, zv, fs + 8 * 2304); }
                        CBAR();
                    }
                    float v[4];
#pragma unroll
                    for (int i = 0; i < 4; ++i) {
                        v[i] = __uint_as_float(p0[i]) + __uint_as_float(p1[i]);
                        v[i] += __builtin_amdgcn_mov_dpp(v[i], 0x128, 0xF, 0xF, true);
                        auto r16 = __builtin_amdgcn_permlane16_swap(__float_as_uint(v[i]), __float_as_uint(v[i]), false, false);
                        v[i] = xhalf_sum(__uint_as_float(r16[0]) + __uint_as_float(r16[1]));
                    }
                    if (X.lane < 8) *(LAS u32x2*)(size_t)(L_KVF + ((X.lane >> 1) & 1) * 544 + 512 + (X.lane >> 2) * 16 + (X.lane & 1) * 8) = u32x2{pk2(v[0], v[1]), pk2(v[2], v[3])};
                }
            } else {
                const unsigned lo = opaque(L_KVF + (X.lane >> 5) * 544 + ((X.u & 1) * 16 + ((X.lane >> 1) & 15)) * 16 + (X.lane & 1) * 8), fh = fs + (X.u & 1) * 1024;
                CBAR();
                STAMPU(sb + 6, 3);
                u32x4 p[16];
#pragma unroll
                for (int i = 0; i < 16; ++i) p[i] = ldw(X, X.l16, fh + i * 2304);
#pragma nounroll
                for (unsigned spins = 0; spins < SPIN_CAP; ++spins) {
                    unsigned m = 0;
#pragma unroll
                    for (int i = 0; i < 16; i += 2) m = max(m, umax8(p[i], p[i + 1]));
                    if (!marked(m)) break;
                    COUNT(6);
                    __builtin_amdgcn_s_sleep(KVP_BACKOFF);
                    CBAR();
#pragma unroll
                    for (int i = 0; i < 16; ++i) if (marked(umax4(p[i]))) p[i] = ldw(X, X.l16, fh + i * 2304);
                    CBAR();
                }
                STAMPU(sb + 10, 3);
                float v[4];
#pragma unroll
                for (int j = 0; j < 4; ++j) {
                    float a = __uint_as_float(p[0][j]), b = __uint_as_float(p[8][j]);
#pragma unroll
                    for (int i = 1; i < 8; ++i) { a = opaque(a + __uint_as_float(p[i][j])); b = opaque(b + __uint_as_float(p[8 + i][j])); }
                    v[j] = a + b;
                }
                *(LAS u32x2*)(size_t)lo = u32x2{pk2(v[0], v[1]), pk2(v[2], v[3])};
            }
            pad<27>();
            STAMP(sb + 13);
            vmwait<0>();
            bar();
        }
        CBAR();
        __builtin_amdgcn_s_setprio(0);
        fresh(X);
        {
            const unsigned ob = OFF_O + l * STEP_XO + tile_of(X) * 32768;
            const int wi = X.g * 4 + X.u;
            rendezvous(X, l, [&]() {
                if (!(l & 1)) static_for<0, 5>([&](auto I) {
                    const int k = wi * 5 + I;
                    if (k < 32) dma1<0, 16>(X.ws, X.l16, OFF_W + SZ_W + X.c * 98304 + 65536 + k * 1024, L_W + 65536 + k * 1024);
                });
                if (l == 0) static_for<0, 5>([&](auto I) {
                    const int k = wi * 5 + I;
                    if (k < 32) dma1<0, 16>(X.ws, X.l16, OFF_W + SZ_W + X.c * 98304 + (k >> 2) * 8192 + (k & 3) * 1024, L_W + (k >> 2) * 8192 + (k & 3) * 1024);
                });
                if (l == 1) static_for<0, (OB_TIMED ? 16 : 10)>([&](auto I) {
                    const int k = OB_TIMED ? X.u * 16 + I : wi * 10 + I;
                    if (k < 64) dma1<1024, 16>(X.ws, X.l16, OFF_W + X.c * 98304 + (k >> 1) * 3072 + (k & 1) * 1024, L_W + (k >> 1) * 3072 + (k & 1) * 1024);
                });
            });
            if (X.g == 0) {
                pad<19>();
                u32x4 xo[2], w[3], of[32];
                f32x16 acc;
#pragma nounroll
                for (int pass = 0;; ++pass) {
                    const unsigned rot = ROTO ? X.c & 7 : 0;
                    ld_range<0, OD>(X, of, ob, rot);
                    unsigned wo = opaque(L_WO + X.l16 + rot * 4096);
                    w[0] = lds128(wo); w[1] = lds128(wo + 1024);
                    STAMP(sb + ((l & 1) ? 14 : 18));
                    CBAR();
                    acc = zero16();
                    static_for<0, 32>([&](auto S) {
                        constexpr int s = S;
                        acc = mfma(w[s % 3], of[s], acc);
                        PIN(acc);
                        if constexpr (s + 2 < 32) {
                            if (((s + 2) & 3) == 0) wo = opaque(L_WO + X.l16 + (((s + 2) >> 2) ^ rot) * 4096);
                            w[(s + 2) % 3] = lds128(wo + ((s + 2) & 3) * 1024);
                        }
                        ld_range<oissued(s), oissued(s + 1)>(X, of, ob, rot);
                        if constexpr (oissued(s) < 32 && oissued(s + 1) == 32) {
                            if (l == 0) { const u32x4* p = (const u32x4*)((const unsigned char*)xin + (size_t)(tile_of(X) * 32 + (X.lane & 31)) * 1024 + X.c * 64 + X.hi * 32); xo[0] = p[0]; xo[1] = p[1]; }
                            else { xo[0] = ldw(X, X.l16, fr_so(X, OFF_X + (l - 1) * STEP_XO)); xo[1] = ldw(X, X.l16 + 1024, fr_so(X, OFF_X + (l - 1) * STEP_XO)); }
                        }
                        CBAR(); SBAR();
                    });
                    if (pass || !UNLIKELY(__builtin_amdgcn_ballot_w64(acc[0] != acc[0]) != 0)) break;
                    COUNT(0);
                    settle_x(X, ob);
                }
                pad<20>();
                STAMP(sb + ((l & 1) ? 11 : 4));
                vmwait<0>();
                outproj_finish(X, acc, xo, l, l == 3, out);
                STAMP(sb + ((l & 1) ? 16 : 5));
            } else vmwait<0>();
        }
        if (l == 3) break;
        bar();
        if (l == 0 && X.g == 1 && X.u == 3) flag_store(X, OFF_WFLAG + 32768 + (X.t * 16 + X.c) * 128);
        fresh(X);
        if (X.g == 0) __builtin_amdgcn_s_sleep(DELAY_X);
        CBAR();
        if (l == 1) {
            if (X.g == 0) {
                u32x4 xf[32];
                STAMP(sb + 17);
                front_a<1>(X, xf, 1);
            } else {
                __builtin_amdgcn_s_sleep(DELAY_ST);
                stats_gather(X, 1, 2);
                if (X.u < 3) {
                    __builtin_amdgcn_s_sleep(DELAY_Q3);
                    static_for<0, 11>([&](auto I) { const int k = X.u * 11 + I; if (k < 32) dma1<0, 16>(X.ws, X.l16, OFF_W + X.c * 98304 + k * 3072, L_W + k * 3072); });
                    vmwait<0>(); set_progress(X.u, 36);
                }
                if (X.u == 3) kv_ready(1); else wait_signal(3);
                if (X.u < 2) dma_lin<0, 16>(X, OFF_WO + X.c * 32768 + X.u * 16384, L_WO + X.u * 16384);
                if (X.u < 3) { mark_x(X, 2); mark2(X, fr_so(X, OFF_O + 3 * STEP_XO)); }
                if (X.u == 2) { X.u = 3; mark_x(X, 2); mark2(X, fr_so(X, OFF_O + 3 * STEP_XO)); }
            }
        }
    }
    fresh(X);
    if (X.t == 0 && X.c == 0 && X.u == 0 && X.g == 0) flag_store(X, 0);
}

extern "C" void kernel_launch(void* const* d_in, const int* in_sizes, int n_in, void* d_out, int out_size, void* d_ws, size_t ws_size, hipStream_t stream) {
    (void)in_sizes; (void)n_in; (void)out_size; (void)ws_size;
    (void)hipFuncSetAttribute((const void*)fused_kernel, hipFuncAttributeMaxDynamicSharedMemorySize, LDS_BYTES);
    fused_kernel<<<256, 512, LDS_BYTES, stream>>>((const unsigned short*)d_in[0], (const unsigned short*)d_in[1], (const unsigned short*)d_in[2],
                                                  (const unsigned short*)d_in[4], (const unsigned short*)d_in[5], (unsigned char*)d_out, (unsigned char*)d_ws + WS_SHIFT);
}
```
